```python
import torch

def _softmax(x, axis=-1):
    return torch.softmax(x, dim=axis)

def _gelu(x):
    return torch.nn.functional.gelu(x, approximate='tanh')

def _silu(x):
    return torch.nn.functional.silu(x)

def _sigmoid(x):
    return torch.sigmoid(x)

def _layernorm(x, axis=-1, eps=1e-05):
    m = x.mean(dim=axis, keepdim=True)
    v = x.var(dim=axis, keepdim=True, unbiased=False)
    return (x - m) / torch.sqrt(v + eps)

def _scatter_add(tgt, idx, src, axis=0):
    return tgt.clone().index_add_(axis, idx, src)

def _segment_sum(src, seg, n):
    out = torch.zeros((n,) + src.shape[1:], dtype=src.dtype)
    return out.index_add_(0, seg, src)

def _pad(x, pw):
    flat = tuple((v for pair in reversed(pw) for v in pair))
    return torch.nn.functional.pad(x, flat)
kernel_name = 'adaLN_cond__memory_aug_recurrent_mem_tr__moe_arch_mod__prompt'

def setup_inputs():

    _w = lambda a: (lambda t: t.to(torch.bfloat16) if t.is_floating_point() else t)(torch.as_tensor(a))

    def _A_setup():
        B, S, Dm = (1, 2048, 512)
        H, Dh = (32, 128)
        M, seg = (8, 32)
        seg = min(seg, S)
        S = S // seg * seg
        return {'x': _w((lambda *s: torch.randn(*s, dtype=torch.bfloat16) * 0.05)(B, S, Dm)), 'mem0': _w((lambda *s: torch.randn(*s, dtype=torch.bfloat16) * 0.4)(B, M, Dm)), 'Wq': _w((lambda *s: torch.randn(*s, dtype=torch.bfloat16) * 3.2)(Dm, H, Dh) * Dm ** (-0.5)), 'Wk': _w((lambda *s: torch.randn(*s, dtype=torch.bfloat16) * 3.2)(Dm, H, Dh) * Dm ** (-0.5)), 'Wv': _w((lambda *s: torch.randn(*s, dtype=torch.bfloat16) * 0.4)(Dm, H, Dh) * Dm ** (-0.5)), 'Wo': _w((lambda *s: torch.randn(*s, dtype=torch.bfloat16) * 0.4)(H, Dh, Dm) * (H * Dh) ** (-0.5)), 'seg_len': seg}
    _A_in = _A_setup()

    def _B_setup():
        B, S, D, F, C = (1, 2048, 512, 1376, 2)
        return {'x': _w((lambda *s: torch.randn(*s, dtype=torch.bfloat16) * 0.05)(B, S, D)), 'w_r': _w((lambda *s: torch.randn(*s, dtype=torch.bfloat16) * 0.05)(D)), 'w_gate': _w((lambda *s: torch.randn(*s, dtype=torch.bfloat16) * 0.05)(D, F)), 'w_up': _w((lambda *s: torch.randn(*s, dtype=torch.bfloat16) * 0.05)(D, F)), 'w_down': _w((lambda *s: torch.randn(*s, dtype=torch.bfloat16) * 0.05)(F, D))}
    _B_in = _B_setup()
    return {'A_x': _A_in['x'], 'A_mem0': _A_in['mem0'], 'A_Wq': _A_in['Wq'], 'A_Wk': _A_in['Wk'], 'A_Wv': _A_in['Wv'], 'A_Wo': _A_in['Wo'], 'A_seg_len': _A_in['seg_len'], 'B_x': _B_in['x'], 'B_w_r': _B_in['w_r'], 'B_w_gate': _B_in['w_gate'], 'B_w_up': _B_in['w_up'], 'B_w_down': _B_in['w_down'], 'c': _w((lambda *s: torch.randn(*s, dtype=torch.bfloat16) * 0.05)(1, 512) * 8.0), 'w_mod': _w((lambda *s: torch.randn(*s, dtype=torch.bfloat16) * 0.05)(512, 6 * 512) * 512 ** (-0.5) * 8.0)}

def reference(A_x, A_mem0, A_Wq, A_Wk, A_Wv, A_Wo, A_seg_len, B_x, B_w_r, B_w_gate, B_w_up, B_w_down, c, w_mod):

    def _A_ref(x, mem0, Wq, Wk, Wv, Wo, seg_len):
        B, S, Dm = x.shape
        H, Dh = (Wq.shape[1], Wq.shape[2])
        M = mem0.shape[1]
        scale = Dh ** (-0.5)
        n_seg = S // seg_len
        L = M + seg_len
        idx = torch.arange(L)
        mask = (idx[:, None] < M) | (idx[:, None] >= idx[None, :])
        mem = mem0
        outs = []
        for s in range(n_seg):
            xs = x[:, s * seg_len:(s + 1) * seg_len, :].clone()
            h = torch.concatenate([mem, xs], axis=1)
            q = torch.einsum('btd,dhe->bhte', h, Wq)
            k = torch.einsum('btd,dhe->bhte', h, Wk)
            v = torch.einsum('btd,dhe->bhte', h, Wv)
            scores = torch.einsum('bhqe,bhke->bhqk', q, k) * scale
            scores = torch.where(mask[None, None], scores, -1000000000.0)
            p = _softmax(scores, axis=-1)
            o = torch.einsum('bhqk,bhke->bhqe', p, v)
            o = torch.einsum('bhte,hed->btd', o, Wo)
            mem = o[:, :M, :].clone()
            outs.append(o[:, M:, :])
        return torch.concatenate(outs, axis=1)

    def _B_ref(x, w_r, w_gate, w_up, w_down):
        B, S, D = x.shape
        C = 2
        r = x @ w_r
        idx = torch.argsort(-r, axis=-1)[:, :C]
        idx_e = (lambda x, n, axis: torch.repeat_interleave(x, n, dim=axis))(idx.reshape(B, C, 1), D, axis=2)
        x_sel = (lambda x, i, axis: torch.take_along_dim(x, i, dim=axis))(x, idx_e, axis=1)
        g = _silu(x_sel @ w_gate)
        u = x_sel @ w_up
        y = g * u @ w_down
        r_sel = _sigmoid((lambda x, i, axis: torch.take_along_dim(x, i, dim=axis))(r, idx, axis=1))
        y = y * r_sel.reshape(B, C, 1)
        b_off = (torch.arange(B) * S).reshape(B, 1)
        flat_idx = (idx + b_off).reshape(B * C)
        zero = (x * 0.0).reshape(B * S, D)
        upd = _scatter_add(zero, flat_idx, y.reshape(B * C, D), axis=0)
        return x + upd.reshape(B, S, D)
    x = A_x
    mod = (_silu(c) @ w_mod).reshape(c.shape[0], 6, 1, -1)
    sa, ca, ga, sb, cb, gb = (mod[:, 0].clone(), mod[:, 1].clone(), mod[:, 2].clone(), mod[:, 3].clone(), mod[:, 4].clone(), mod[:, 5].clone())
    h = _layernorm(x, axis=-1) * (1 + ca) + sa
    x = x + ga * _A_ref(h, A_mem0, A_Wq, A_Wk, A_Wv, A_Wo, A_seg_len)
    h = _layernorm(x, axis=-1) * (1 + cb) + sb
    x = x + gb * _B_ref(h, B_w_r, B_w_gate, B_w_up, B_w_down)
    return x
```

```cpp
#include <hip/hip_runtime.h>
#include <hip/hip_bf16.h>
typedef unsigned short u16;
__device__ __forceinline__ float bf2f(u16 b) { return __uint_as_float((unsigned)b << 16); }
__device__ __forceinline__ u16 f2bf(float f) { return __builtin_bit_cast(u16, (__bf16)f); }
__device__ __forceinline__ float rbf(float f) { return bf2f(f2bf(f)); }
constexpr int S = 2048, D = 512, H = 32, E = 128, M = 8, SEG = 32, NSEG = 64, F = 1376, HE = 4096;

__global__ void k_mod(const u16* c, const u16* wmod, float* mod) {
  int j = blockIdx.x * 256 + threadIdx.x;
  float a = 0;
  for (int k = 0; k < D; ++k) { float x = bf2f(c[k]); float s = rbf(x / (1.f + __expf(-x))); a += s * bf2f(wmod[k * 3072 + j]); }
  mod[j] = rbf(a);
}
__device__ float wsum(float v) { for (int m = 32; m; m >>= 1) v += __shfl_xor(v, m); return v; }
__global__ void k_ln(const u16* x, const float* shift, const float* scale, u16* h) {
  int row = blockIdx.x, l = threadIdx.x;
  float v[8]; float s = 0;
  for (int i = 0; i < 8; ++i) { v[i] = bf2f(x[row * D + l * 8 + i]); s += v[i]; }
  float mu = wsum(s) / D; float q = 0;
  for (int i = 0; i < 8; ++i) { float d = v[i] - mu; q += d * d; }
  float rs = rsqrtf(wsum(q) / D + 1e-5f);
  for (int i = 0; i < 8; ++i) { int d = l * 8 + i; h[row * D + d] = f2bf((v[i] - mu) * rs * (1.f + scale[d]) + shift[d]); }
}
__global__ void k_gemm(const u16* A, const u16* B, u16* C, int Mr, int N, int K) {
  __shared__ float As[32][33], Bs[32][65];
  int tx = threadIdx.x & 63, ty = threadIdx.x >> 6;
  int n0 = blockIdx.x * 64, m0 = blockIdx.y * 32;
  float acc[8] = {0};
  for (int k0 = 0; k0 < K; k0 += 32) {
    for (int i = threadIdx.x; i < 1024; i += 256) { int r = i >> 5, cc = i & 31; As[r][cc] = (m0 + r < Mr) ? bf2f(A[(size_t)(m0 + r) * K + k0 + cc]) : 0.f; }
    for (int i = threadIdx.x; i < 2048; i += 256) { int r = i >> 6, cc = i & 63; Bs[r][cc] = bf2f(B[(size_t)(k0 + r) * N + n0 + cc]); }
    __syncthreads();
    for (int k = 0; k < 32; ++k) { float b = Bs[k][tx]; for (int i = 0; i < 8; ++i) acc[i] += As[ty * 8 + i][k] * b; }
    __syncthreads();
  }
  for (int i = 0; i < 8; ++i) if (m0 + ty * 8 + i < Mr) C[(size_t)(m0 + ty * 8 + i) * N + n0 + tx] = f2bf(acc[i]);
}
__global__ void k_attn(const u16* Qp, const u16* Km, const u16* Vm, const u16* Kt, const u16* Vt, u16* O, int nq, int causal, int segstride) {
  int h = blockIdx.x, s = blockIdx.y, t = threadIdx.x;
  __shared__ float Ks[40][129], Vs[40][129], P[40], Qs[128];
  const u16* km = Km + (size_t)s * segstride * M * HE; const u16* vm = Vm + (size_t)s * segstride * M * HE;
  const u16* kt = Kt + (size_t)s * SEG * HE; const u16* vt = Vt + (size_t)s * SEG * HE;
  for (int j = 0; j < 40; ++j) { Ks[j][t] = bf2f(j < M ? km[j * HE + h * E + t] : kt[(j - M) * HE + h * E + t]); Vs[j][t] = bf2f(j < M ? vm[j * HE + h * E + t] : vt[(j - M) * HE + h * E + t]); }
  for (int i = 0; i < nq; ++i) {
    __syncthreads();
    Qs[t] = bf2f(Qp[((size_t)s * nq + i) * HE + h * E + t]);
    __syncthreads();
    if (t < 40) { float a = 0; for (int e = 0; e < E; ++e) a += Qs[e] * Ks[t][e]; a *= 0.08838834764831845f; if (causal && t > i + M) a = -1e9f; P[t] = a; }
    __syncthreads();
    float m = -1e30f; for (int j = 0; j < 40; ++j) m = fmaxf(m, P[j]);
    float l = 0, o = 0; for (int j = 0; j < 40; ++j) { float p = __expf(P[j] - m); l += p; o += p * Vs[j][t]; }
    O[((size_t)s * nq + i) * HE + h * E + t] = f2bf(o / l);
  }
}
__global__ void k_res(const u16* x, const u16* a, const float* ga, u16* x1) {
  int i = blockIdx.x * 256 + threadIdx.x; x1[i] = f2bf(bf2f(x[i]) + rbf(ga[i & 511] * bf2f(a[i])));
}
__global__ void k_router(const u16* h2, const u16* wr, float* r) {
  int row = blockIdx.x, l = threadIdx.x; float s = 0;
  for (int i = 0; i < 8; ++i) s += bf2f(h2[row * D + l * 8 + i]) * bf2f(wr[l * 8 + i]);
  s = wsum(s); if (l == 0) r[row] = rbf(s);
}
__global__ void k_top2(const float* r, int* idx, const u16* h2, u16* xsel) {
  __shared__ int sidx[2];
  if (threadIdx.x == 0) { int b0 = 0; for (int i = 1; i < S; ++i) if (r[i] > r[b0]) b0 = i; int b1 = b0 == 0 ? 1 : 0; for (int i = 0; i < S; ++i) if (i != b0 && r[i] > r[b1]) b1 = i; sidx[0] = b0; sidx[1] = b1; idx[0] = b0; idx[1] = b1; }
  __syncthreads();
  for (int i = threadIdx.x; i < 1024; i += 256) xsel[i] = h2[sidx[i >> 9] * D + (i & 511)];
}
__global__ void k_ffn1(const u16* xsel, const u16* wg, const u16* wu, float* act) {
  int f = blockIdx.x * 32 + (threadIdx.x & 31), c = threadIdx.x >> 5; if (f >= F) return;
  float g = 0, u = 0;
  for (int k = 0; k < D; ++k) { float x = bf2f(xsel[c * D + k]); g += x * bf2f(wg[k * F + f]); u += x * bf2f(wu[k * F + f]); }
  g = rbf(g); u = rbf(u); g = rbf(g / (1.f + __expf(-g))); act[c * F + f] = rbf(g * u);
}
__global__ void k_ffn2(const float* act, const u16* wd, const float* r, const int* idx, float* y) {
  int d = blockIdx.x * 32 + (threadIdx.x & 31), c = threadIdx.x >> 5; float a = 0;
  for (int k = 0; k < F; ++k) a += act[c * F + k] * bf2f(wd[k * D + d]);
  float rs = rbf(1.f / (1.f + __expf(-r[idx[c]]))); y[c * D + d] = rbf(rbf(a) * rs);
}
__global__ void k_out(const u16* x1, const u16* h2, const float* gb, const int* idx, const float* y, u16* out) {
  int i = blockIdx.x * 256 + threadIdx.x; int row = i >> 9, d = i & 511; float b = bf2f(h2[i]);
  if (row == idx[0]) b = rbf(b + y[d]); else if (row == idx[1]) b = rbf(b + y[D + d]);
  out[i] = f2bf(bf2f(x1[i]) + rbf(gb[d] * b));
}
extern "C" void kernel_launch(void* const* d_in, const int* in_sizes, int n_in, void* d_out, int out_size, void* d_ws, size_t ws_size, hipStream_t st) {
  const u16 *x = (const u16*)d_in[0], *mem0 = (const u16*)d_in[1], *Wq = (const u16*)d_in[2], *Wk = (const u16*)d_in[3], *Wv = (const u16*)d_in[4], *Wo = (const u16*)d_in[5];
  const u16 *wr = (const u16*)d_in[8], *wg = (const u16*)d_in[9], *wu = (const u16*)d_in[10], *wd = (const u16*)d_in[11], *c = (const u16*)d_in[12], *wmod = (const u16*)d_in[13];
  char* w = (char*)d_ws; size_t o = 0;
  auto al = [&](size_t n) { char* p = w + o; o += (n + 255) & ~(size_t)255; return p; };
  float* mod = (float*)al(3072 * 4); u16* h1 = (u16*)al(S * D * 2); u16* Q = (u16*)al((size_t)S * HE * 2); u16* K = (u16*)al((size_t)S * HE * 2); u16* V = (u16*)al((size_t)S * HE * 2);
  u16* O = (u16*)al((size_t)S * HE * 2); u16* mem = (u16*)al(65 * M * D * 2); u16* Qm = (u16*)al(M * HE * 2); u16* Km = (u16*)al((size_t)64 * M * HE * 2); u16* Vm = (u16*)al((size_t)64 * M * HE * 2);
  u16* Om = (u16*)al(M * HE * 2); u16* A = (u16*)al(S * D * 2); u16* x1 = (u16*)al(S * D * 2); u16* h2 = (u16*)al(S * D * 2); float* r = (float*)al(S * 4); int* idx = (int*)al(16);
  u16* xsel = (u16*)al(2 * D * 2); float* act = (float*)al(2 * F * 4); float* y = (float*)al(2 * D * 4);
  k_mod<<<12, 256, 0, st>>>(c, wmod, mod);
  k_ln<<<S, 64, 0, st>>>(x, mod, mod + 512, h1);
  k_gemm<<<dim3(64, 64), 256, 0, st>>>(h1, Wq, Q, S, HE, D);
  k_gemm<<<dim3(64, 64), 256, 0, st>>>(h1, Wk, K, S, HE, D);
  k_gemm<<<dim3(64, 64), 256, 0, st>>>(h1, Wv, V, S, HE, D);
  hipMemcpyAsync(mem, mem0, M * D * 2, hipMemcpyDeviceToDevice, st);
  for (int s = 0; s < NSEG; ++s) {
    const u16* ms = mem + s * M * D;
    k_gemm<<<dim3(64, 1), 256, 0, st>>>(ms, Wk, Km + (size_t)s * M * HE, M, HE, D);
    k_gemm<<<dim3(64, 1), 256, 0, st>>>(ms, Wv, Vm + (size_t)s * M * HE, M, HE, D);
    if (s == NSEG - 1) break;
    k_gemm<<<dim3(64, 1), 256, 0, st>>>(ms, Wq, Qm, M, HE, D);
    k_attn<<<dim3(H, 1), 128, 0, st>>>(Qm, Km + (size_t)s * M * HE, Vm + (size_t)s * M * HE, K + (size_t)s * SEG * HE, V + (size_t)s * SEG * HE, Om, M, 0, 0);
    k_gemm<<<dim3(8, 1), 256, 0, st>>>(Om, Wo, mem + (s + 1) * M * D, M, D, HE);
  }
  k_attn<<<dim3(H, NSEG), 128, 0, st>>>(Q, Km, Vm, K, V, O, SEG, 1, 1);
  k_gemm<<<dim3(8, 64), 256, 0, st>>>(O, Wo, A, S, D, HE);
  k_res<<<S * D / 256, 256, 0, st>>>(x, A, mod + 1024, x1);
  k_ln<<<S, 64, 0, st>>>(x1, mod + 1536, mod + 2048, h2);
  k_router<<<S, 64, 0, st>>>(h2, wr, r);
  k_top2<<<1, 256, 0, st>>>(r, idx, h2, xsel);
  k_ffn1<<<43, 64, 0, st>>>(xsel, wg, wu, act);
  k_ffn2<<<16, 64, 0, st>>>(act, wd, r, idx, y);
  k_out<<<S * D / 256, 256, 0, st>>>(x1, h2, mod + 2560, idx, y, (u16*)d_out);
}
```

```cpp
#include <hip/hip_runtime.h>
#include <hip/hip_bf16.h>

typedef unsigned short u16;
typedef unsigned v4u __attribute__((ext_vector_type(4)));
typedef unsigned v2u __attribute__((ext_vector_type(2)));
typedef unsigned v8u __attribute__((ext_vector_type(8)));
typedef float f32x4 __attribute__((ext_vector_type(4)));
typedef float f32x2 __attribute__((ext_vector_type(2)));
typedef __bf16 bf16x8 __attribute__((ext_vector_type(8)));
typedef __bf16 bf16x2 __attribute__((ext_vector_type(2)));
typedef short s16x4 __attribute__((ext_vector_type(4)));
typedef __amdgpu_buffer_rsrc_t rsrc_t;
#define LAS __attribute__((address_space(3)))
#define DEV __device__ __forceinline__
#define LBAR() asm volatile("s_waitcnt lgkmcnt(0)\n\ts_barrier" ::: "memory")
#define DRAIN() asm volatile("s_waitcnt vmcnt(0)" ::: "memory")
#define CFENCE() asm volatile("" ::: "memory")
#define BATCH() do { CFENCE(); __builtin_amdgcn_sched_barrier(0); } while (0)
constexpr int SC1 = 16;

constexpr unsigned WS_BYTES = 0x10000000u;
constexpr unsigned C_READY = 0, C_VOTE = 256, C_EPOCH = 1024, C_KVF = 4096, C_H1F = 12288, C_KMF = 16384, C_PF = 81920, C_MOD = 36864, C_R = 49152,
                   C_RF = 131072, C_YF = 69632, C_Y = 262144;
#define SPECV 52
constexpr int SPEC_SEGS = SPECV;
constexpr int SEG_W = 20;
#define SEG4V 56
constexpr int SEG_4 = SEG4V;
constexpr unsigned WS_COMM = 2u << 20, WS_KM = 4u << 20, WS_H1 = 12u << 20, WS_X1 = 14u << 20, WS_H2 = 16u << 20, WS_KV = 64u << 20, WS_PART = 128u << 20;
#define VREPS 6
#define KVX 0
#define PX 0
constexpr int RS = 30720, O_MEM = 28672, L_KV = 122880, L_QK = 155648, QKP = 288, L_VI = L_QK + 16 * QKP, L_END = 162816;
#define MA_SLEEP 4
#define KM63_DELAY 2
#define KM62_DELAY 20
#define P63_DELAY 16
#define KM_SLEEP 16
#define LM_SLEEP 3
constexpr unsigned SPIN_CAP = 1u << 17;

struct Args { const u16 *x, *mem0, *Wq, *Wk, *Wv, *Wo, *wr, *wg, *wu, *wd, *c, *wmod; u16* out; char* ws; };

typedef const __attribute__((address_space(4))) Args* ArgP;
DEV ArgP fresh(ArgP p) { asm volatile("" : "+s"(p)); return p; }
DEV int opq(int v) { asm volatile("" : "+s"(v)); return v; }
DEV int lane_id() { int l; asm volatile("v_mbcnt_lo_u32_b32 %0, -1, 0\n\tv_mbcnt_hi_u32_b32 %0, -1, %0" : "=v"(l)); return l; }
DEV unsigned lo2(unsigned a, unsigned b) { return __builtin_amdgcn_perm(b, a, 0x05040100u); }
DEV unsigned hi2(unsigned a, unsigned b) { return __builtin_amdgcn_perm(b, a, 0x07060302u); }
DEV unsigned pk2(float a, float b) { f32x2 v = {a, b}; return __builtin_bit_cast(unsigned, __builtin_convertvector(v, bf16x2)); }
DEV float blo(unsigned u) { return __uint_as_float(u << 16); }
DEV float bhi(unsigned u) { return __uint_as_float(u & 0xffff0000u); }
DEV float rbf(float f) { return blo(pk2(f, f)); }
DEV v4u ldsr(LAS char* p) { return *(LAS v4u*)p; }
DEV void ldsw(LAS char* p, v4u v) { *(LAS v4u*)p = v; }
DEV v2u ldstr(LAS char* p) { return __builtin_bit_cast(v2u, __builtin_amdgcn_ds_read_tr16_b64_v4i16((LAS s16x4*)p)); }
DEV f32x4 mf32(v4u a, v4u b, f32x4 c) { return __builtin_amdgcn_mfma_f32_16x16x32_bf16(__builtin_bit_cast(bf16x8, a), __builtin_bit_cast(bf16x8, b), c, 0, 0, 0); }
DEV f32x4 mf16(v2u a, v2u b, f32x4 c) { return __builtin_amdgcn_mfma_f32_16x16x16bf16_1k(__builtin_bit_cast(s16x4, a), __builtin_bit_cast(s16x4, b), c, 0, 0, 0); }
DEV v4u gld4(rsrc_t r, unsigned o) { return __builtin_amdgcn_raw_buffer_load_b128(r, o, 0, SC1); }
DEV v4u gld4s(rsrc_t r, unsigned o, unsigned so) { return __builtin_amdgcn_raw_buffer_load_b128(r, o, so, SC1); }
DEV unsigned gld1(rsrc_t r, unsigned o) { return __builtin_amdgcn_raw_buffer_load_b32(r, o, 0, SC1); }
DEV rsrc_t mkr(const void* p, unsigned bytes) { return __builtin_amdgcn_make_buffer_rsrc((void*)p, 0, bytes, 0x00020000); }
DEV v4u ild4(rsrc_t r, unsigned v, unsigned so) { return __builtin_amdgcn_raw_buffer_load_b128(r, v, so, 0); }
DEV unsigned ild1(rsrc_t r, unsigned v, unsigned so) { return __builtin_amdgcn_raw_buffer_load_b32(r, v, so, 0); }
DEV void gst2(rsrc_t r, unsigned o, unsigned a, unsigned b) { __builtin_amdgcn_raw_buffer_store_b64((v2u){a, b}, r, o, 0, SC1); }
DEV void gst4(rsrc_t r, unsigned o, v4u v) { __builtin_amdgcn_raw_buffer_store_b128(v, r, o, 0, SC1); }
DEV void gst1(rsrc_t r, unsigned o, unsigned v) { __builtin_amdgcn_raw_buffer_store_b32(v, r, o, 0, SC1); }
DEV unsigned long long clk() { unsigned long long t; asm volatile("s_memtime %0\n\ts_waitcnt lgkmcnt(0)" : "=s"(t) :: "memory"); return t; }
template <int C> DEV float dppf(float x) { return __int_as_float(__builtin_amdgcn_mov_dpp(__float_as_int(x), C, 0xf, 0xf, true)); }
template <int C> DEV unsigned dppu(unsigned x) { return (unsigned)__builtin_amdgcn_mov_dpp((int)x, C, 0xf, 0xf, true); }
DEV float xsum4(float x) {
  auto s = __builtin_amdgcn_permlane16_swap(__float_as_uint(x), __float_as_uint(x), false, false);
  x = __uint_as_float(s[0]) + __uint_as_float(s[1]);
  auto t = __builtin_amdgcn_permlane32_swap(__float_as_uint(x), __float_as_uint(x), false, false);
  return __uint_as_float(t[0]) + __uint_as_float(t[1]);
}
DEV float xmax4(float x) {
  auto s = __builtin_amdgcn_permlane16_swap(__float_as_uint(x), __float_as_uint(x), false, false);
  x = fmaxf(__uint_as_float(s[0]), __uint_as_float(s[1]));
  auto t = __builtin_amdgcn_permlane32_swap(__float_as_uint(x), __float_as_uint(x), false, false);
  return fmaxf(__uint_as_float(t[0]), __uint_as_float(t[1]));
}
DEV float wsum(float x) { x += dppf<0xB1>(x); x += dppf<0x4E>(x); x += dppf<0x141>(x); x += dppf<0x128>(x); return xsum4(x); }
DEV unsigned wmaxu(unsigned x) {
  x = max(x, dppu<0xB1>(x)); x = max(x, dppu<0x4E>(x)); x = max(x, dppu<0x141>(x)); x = max(x, dppu<0x128>(x));
  auto s = __builtin_amdgcn_permlane16_swap(x, x, false, false); x = max(s[0], s[1]);
  auto t = __builtin_amdgcn_permlane32_swap(x, x, false, false); return max(t[0], t[1]);
}
DEV float sigm(float x) { return __builtin_amdgcn_rcpf(1.f + __expf(-x)); }
DEV v4u mksrd(const void* p, unsigned bytes) {
  const unsigned long long a = (unsigned long long)p;
  v4u r = {(unsigned)__builtin_amdgcn_readfirstlane((unsigned)a), (unsigned)__builtin_amdgcn_readfirstlane((unsigned)(a >> 32) & 0xffffu), bytes, 0x00020000u};
  return r;
}
DEV void dma16(v4u srd, unsigned voff, unsigned soff, unsigned ldsaddr) {
  asm volatile("s_mov_b32 m0, %3\n\ts_nop 0\n\tbuffer_load_dwordx4 %0, %1, %2 offen lds" :: "v"(voff), "s"(srd), "s"(soff), "s"(ldsaddr) : "m0", "memory");
}
DEV void dma16s(v4u srd, unsigned voff, unsigned soff, unsigned ldsaddr) {
  asm volatile("s_mov_b32 m0, %3\n\ts_nop 0\n\tbuffer_load_dwordx4 %0, %1, %2 offen sc1 lds" :: "v"(voff), "s"(srd), "s"(soff), "s"(ldsaddr) : "m0", "memory");
}
template <int SLEEP> DEV void wait_ge(rsrc_t rs, unsigned off, unsigned E, unsigned need, unsigned& budget) {
  for (;;) {
    CFENCE();
    const unsigned v = gld1(rs, off) - E - need;
    if (__all(v <= 64u - need)) break;
    if (++budget > SPIN_CAP) break;
    __builtin_amdgcn_s_sleep(SLEEP);
  }
  CFENCE();
}
DEV void rest_until(int w, rsrc_t rs, unsigned off, unsigned E, unsigned need, unsigned& budget) {
  if (w == 0) wait_ge<100>(rs, off, E, need, budget);
  LBAR();
}
DEV unsigned near_class(rsrc_t rs, unsigned E, unsigned& budget) {
  unsigned v;
  for (;;) {
    CFENCE();
    v = gld1(rs, C_VOTE + (lane_id() & 15) * 4) - E;
    if (__all(v - 1u <= 1u) || ++budget > SPIN_CAP) break;
    __builtin_amdgcn_s_sleep(4);
  }
  return __builtin_popcount((unsigned)__ballot(v == 2u) & 0xffffu) > 8 ? 8192u : 0u;
}
DEV unsigned epoch(unsigned ev) { asm volatile("" : "+v"(ev) :: "memory"); return __builtin_amdgcn_readfirstlane(ev) + 128u; }
DEV unsigned vmax4(v4u a) { return max(max(a.x, a.y), max(a.z, a.w)); }
DEV v4u clamp4(v4u a) { return (v4u){min(a.x, 0xFFFFFFFEu), min(a.y, 0xFFFFFFFEu), min(a.z, 0xFFFFFFFEu), min(a.w, 0xFFFFFFFEu)}; }
DEV void ln_row(const float (&v)[8], float (&o)[8]) {
  float s = 0.f;
#pragma unroll
  for (int i = 0; i < 8; ++i) s += v[i];
  const float mu = wsum(s) * (1.f / 512.f);
  float q = 0.f;
#pragma unroll
  for (int i = 0; i < 8; ++i) { const float d = v[i] - mu; q += d * d; }
  const float var = rbf(wsum(q) * (1.f / 512.f));
  const float inv = __builtin_amdgcn_rcpf(rbf(__builtin_amdgcn_sqrtf(rbf(var + 1e-5f))));
  const float m = rbf(mu);
#pragma unroll
  for (int i = 0; i < 8; ++i) o[i] = rbf(rbf(v[i] - m) * inv);
}
DEV void unpack8(v4u u, float (&v)[8]) {
#pragma unroll
  for (int i = 0; i < 4; ++i) { v[2 * i] = blo(u[i]); v[2 * i + 1] = bhi(u[i]); }
}
DEV unsigned cmap(unsigned off, unsigned par) { return WS_COMM + ((off >> 13) << 14) + par + (off & 8191u); }


DEV void chain_role(ArgP Ap, int w, int ci, rsrc_t rs
) {
  const int lane = lane_id(), tid = w * 64 + lane;
  const int n = lane & 15, g = lane >> 4;
  const int h = ci >> 1, c = ci & 1;
  {
    const v4u f = {~0u, ~0u, ~0u, ~0u};
    for (unsigned par = 0; par <= 8192; par += 8192) {
      gst4(rs, cmap(ci * 4096 + tid * 16, par) + 0x100000, f);
      gst4(rs, cmap(ci * 4096 + tid * 16, par) + 0x180000, f);
      if (tid < 128) gst4(rs, cmap(262144 + (tid >> 3) * 8192 + ci * 128 + (tid & 7) * 16, par), f);
    }
    gst4(rs, WS_KM + (h * 64 + 62 + (tid >> 7)) * 4096 + c * 2048 + (tid & 127) * 16, f);
  }
  const int rho = g & 1, gh = g >> 1, yp = lane & 7;
  v8u r0, r1, r2, r3, r4;
  r0[0] = n < 8 ? 0x4444u : 0xEEEEu;
  r0[1] = n < 8 ? 0x8888u : 0xDDDDu;
  r0[2] = w * RS + lane * 16;
  r0[3] = ((w + 1) & 3) * RS + O_MEM + lane * 16;
  r0[4] = ((w + 2) & 3) * RS + O_MEM + lane * 16;
  r0[5] = ((w + 3) & 3) * RS + O_MEM + lane * 16;
  r0[6] = L_QK + (8 * (w >> 1) + 4 * rho + 2 * gh) * QKP + (64 * (w & 1) + 4 * n) * 2;
  r0[7] = L_QK + (n & 7) * QKP + g * 16;
  r1[0] = L_VI + (4 * rho + 2 * gh) * QKP + (32 * w + 2 * n) * 2;
  r1[1] = L_KV + lane * 16;
#define SWZ 1
  r1[3] = cmap((32 * c + 8 * w + (n >> 1)) * 4096 + ((h + SWZ * (8 * w + (n >> 1)) + SWZ * c * 8) & 31) * 128 + (2 * g + (n & 1)) * 16, 0x100000);
  const unsigned gx0 = cmap(262144 + (ci >> 4) * 8192 + w * 2048, 0);
  const unsigned kvf = C_KVF + ((n >> 3) * 64 + 2 * h + ((n >> 2) & 1)) * 64 + (n & 3) * 4;
  r1[4] = gx0 + lane * 16;
  r1[5] = w < 2 ? kvf : w == 2 ? WS_KM + h * (64 * 4096) + c * 2048 + lane * 16 : cmap(ci * 4096 + lane * 16, 0x100000);
  r1[6] = w == 2 ? (c ? L_VI : L_QK + 8 * QKP) + g * QKP + n * 16 : cmap(262144 + (n >> 3) * 8192 + ci * 128 + (4 * ((yp >> 1) & 1) + g) * 16 + (2 * (yp & 1) + (yp >> 2)) * 4, 0);
  r1[2] = (n & 3) < 2 ? (0x3f80u << (16 * (n & 1))) : 0u;
  const int ecol = 32 * w + 16 * ((n >> 2) & 1) + 8 * ((n >> 1) & 1) + 4 * (n >> 3) + 2 * (n & 1);
  { const int p = lane >> 3, q = lane >> 2;
    r3[0] = (4 * (p & 1) + 2 * (p >> 2) + ((p >> 1) & 1)) * 8192 + (h * 128 + 64 * (w & 1)) * 2 + (lane & 7) * 16;
    r3[1] = ((q & 8) + 4 * (q & 1) + 2 * ((q >> 2) & 1) + ((q >> 1) & 1)) * 8192 + (h * 128 + 32 * w) * 2 + (lane & 3) * 16; }
  r3[2] = (h * 128 + 8 * g) * 1024 + (256 * c + 64 * w + 4 * n) * 2;
  r3[3] = 0;
  const unsigned kv0 = WS_KV + h * (2u << 20) + (w >> 1) * 16384 + (w & 1) * 4096;
  r3[4] = kv0 - gx0;
  r3[5] = kvf;
  r3[6] = C_KMF + ci * 128;
  r3[7] = kv0 + 2 * 32768 - 4 * gx0;
  r2[0] = gh * 2048 + rho * 128 + 8 * n;
  r2[1] = gh * 1024 + rho * 64 + (ecol - 32 * w) * 2;
  for (int k = 2; k < 8; ++k) r2[k] = 0;
  r2[2] = ((h & 1) ^ KVX) * 8192;
  r2[4] = (ci & 15) < 2 ? (ci & 1) * 1024u : 0x40000000u;
  r2[7] = w == 0 ? C_READY + ci * 4 : 0x40000000u;
  r2[5] = w == 1 ? 18u * 16384u : 0x40000000u;
#define PW 3
  r2[6] = w == PW ? 18u * 16384u : 0x40000000u;
  if (w == 1) r1[5] = cmap(262144 + (lane & 3) * 8192 + PW * 2048 + ci * 16, 0);
  for (int k = 0; k < 8; ++k) r4[k] = (lane & 7) * 1024 + (w * 16 + (lane >> 3)) * 16;
  const v4u srd_ws = mksrd(Ap->ws, WS_BYTES), srd_w = mksrd(w < 2 ? Ap->Wq : Ap->Wk, 512 * 4096 * 2), srd_v = mksrd(Ap->Wv, 512 * 4096 * 2), srd_o = mksrd(Ap->Wo, 512 * 4096 * 2);
  if (w == 1) { gst1(rs, r1[5] + r2[5], 0); gst1(rs, r1[5] + r2[5] + 8192, 0); }
  const v4u srd_m = mksrd(Ap->mem0, 8192);
  v4u r1a = {r1[0], r1[1], r1[2], r1[3]}; v2u r1b = {r1[4], r1[5]}; unsigned r1c = r1[6];
  asm volatile(
"s_nop 4\n"
"s_mov_b32 s56, 0x05040100\n"
"s_mov_b32 s57, 0x07060302\n"
"s_mov_b32 s58, 0x3e0293ee\n"
"s_mov_b32 s96, 0x3f803f80\n"
"s_mov_b32 s97, 0x3f803f80\n"
"s_mov_b32 s50, 0x00ff00ff\n"
"s_mov_b32 s51, 0xff00ff00\n"
"s_mov_b32 s52, 0x00ff00ff\n"
"s_mov_b32 s53, 0x00ff00ff\n"
"s_mov_b32 s80, 0xf000f\n"
"s_mov_b32 s81, 0xf000f\n"
"s_mov_b32 s82, 0xf000f0\n"
"s_mov_b32 s83, 0xf000f0\n"
"s_mov_b32 s84, 0xf000f00\n"
"s_mov_b32 s85, 0xf000f00\n"
"s_mov_b32 s86, 0xf000f000\n"
"s_mov_b32 s87, 0xf000f000\n"
"v_readfirstlane_b32 s93, v26\n"
"v_readfirstlane_b32 s37, v28\n"
"s_mul_i32 s60, %11, 30720\n"
"s_lshl_b32 s61, %11, 13\n"
"s_add_u32 s61, s61, 122880\n"
"buffer_load_dword v27, off, %7, 0 offset:1024 sc1\n"
"buffer_load_dwordx4 v[120:123], v64, %12, 0 offen\n"
"buffer_load_dwordx4 v[124:127], v64, %12, 0 offen offset:128\n"
"s_lshl_b32 s54, %11, 1\n"
"s_add_u32 s54, s54, 0\n"
"s_and_b32 s54, s54, 7\n"
"s_lshl_b32 s54, s54, 19\n"
"s_add_u32 m0, s61, 0\n"
"s_add_u32 s55, s54, 0\n"
"buffer_load_dwordx4 v16, %8, s55 offen offset:0 lds\n"
"s_add_u32 s55, s54, 64512\n"
"buffer_load_dwordx4 v16, %8, s55 offen offset:1024 lds\n"
"s_add_u32 s55, s54, 129024\n"
"buffer_load_dwordx4 v16, %8, s55 offen offset:2048 lds\n"
"s_add_u32 s55, s54, 193536\n"
"buffer_load_dwordx4 v16, %8, s55 offen offset:3072 lds\n"
"s_add_u32 m0, s61, 4096\n"
"s_add_u32 s55, s54, 262144\n"
"buffer_load_dwordx4 v16, %8, s55 offen offset:0 lds\n"
"s_add_u32 s55, s54, 326656\n"
"buffer_load_dwordx4 v16, %8, s55 offen offset:1024 lds\n"
"s_add_u32 s55, s54, 391168\n"
"buffer_load_dwordx4 v16, %8, s55 offen offset:2048 lds\n"
"s_add_u32 s55, s54, 455680\n"
"buffer_load_dwordx4 v16, %8, s55 offen offset:3072 lds\n"
"s_lshl_b32 s54, %11, 1\n"
"s_add_u32 s54, s54, 1\n"
"s_and_b32 s54, s54, 7\n"
"s_lshl_b32 s54, s54, 19\n"
"s_add_u32 m0, s60, 0\n"
"s_add_u32 s55, s54, 0\n"
"buffer_load_dwordx4 v16, %8, s55 offen offset:0 lds\n"
"s_add_u32 s55, s54, 64512\n"
"buffer_load_dwordx4 v16, %8, s55 offen offset:1024 lds\n"
"s_add_u32 s55, s54, 129024\n"
"buffer_load_dwordx4 v16, %8, s55 offen offset:2048 lds\n"
"s_add_u32 s55, s54, 193536\n"
"buffer_load_dwordx4 v16, %8, s55 offen offset:3072 lds\n"
"s_add_u32 m0, s60, 4096\n"
"s_add_u32 s55, s54, 262144\n"
"buffer_load_dwordx4 v16, %8, s55 offen offset:0 lds\n"
"s_add_u32 s55, s54, 326656\n"
"buffer_load_dwordx4 v16, %8, s55 offen offset:1024 lds\n"
"s_add_u32 s55, s54, 391168\n"
"buffer_load_dwordx4 v16, %8, s55 offen offset:2048 lds\n"
"s_add_u32 s55, s54, 455680\n"
"buffer_load_dwordx4 v16, %8, s55 offen offset:3072 lds\n"
"s_lshl_b32 s54, %11, 1\n"
"s_add_u32 s54, s54, 2\n"
"s_and_b32 s54, s54, 7\n"
"s_lshl_b32 s54, s54, 19\n"
"s_add_u32 m0, s60, 8192\n"
"s_add_u32 s55, s54, 0\n"
"buffer_load_dwordx4 v16, %8, s55 offen offset:0 lds\n"
"s_add_u32 s55, s54, 64512\n"
"buffer_load_dwordx4 v16, %8, s55 offen offset:1024 lds\n"
"s_add_u32 s55, s54, 129024\n"
"buffer_load_dwordx4 v16, %8, s55 offen offset:2048 lds\n"
"s_add_u32 s55, s54, 193536\n"
"buffer_load_dwordx4 v16, %8, s55 offen offset:3072 lds\n"
"s_add_u32 m0, s60, 12288\n"
"s_add_u32 s55, s54, 262144\n"
"buffer_load_dwordx4 v16, %8, s55 offen offset:0 lds\n"
"s_add_u32 s55, s54, 326656\n"
"buffer_load_dwordx4 v16, %8, s55 offen offset:1024 lds\n"
"s_add_u32 s55, s54, 391168\n"
"buffer_load_dwordx4 v16, %8, s55 offen offset:2048 lds\n"
"s_add_u32 s55, s54, 455680\n"
"buffer_load_dwordx4 v16, %8, s55 offen offset:3072 lds\n"
"s_lshl_b32 s54, %11, 1\n"
"s_add_u32 s54, s54, 3\n"
"s_and_b32 s54, s54, 7\n"
"s_lshl_b32 s54, s54, 19\n"
"s_add_u32 m0, s60, 16384\n"
"s_add_u32 s55, s54, 0\n"
"buffer_load_dwordx4 v16, %8, s55 offen offset:0 lds\n"
"s_add_u32 s55, s54, 64512\n"
"buffer_load_dwordx4 v16, %8, s55 offen offset:1024 lds\n"
"s_add_u32 s55, s54, 129024\n"
"buffer_load_dwordx4 v16, %8, s55 offen offset:2048 lds\n"
"s_add_u32 s55, s54, 193536\n"
"buffer_load_dwordx4 v16, %8, s55 offen offset:3072 lds\n"
"s_add_u32 m0, s60, 20480\n"
"s_add_u32 s55, s54, 262144\n"
"buffer_load_dwordx4 v16, %8, s55 offen offset:0 lds\n"
"s_add_u32 s55, s54, 326656\n"
"buffer_load_dwordx4 v16, %8, s55 offen offset:1024 lds\n"
"s_add_u32 s55, s54, 391168\n"
"buffer_load_dwordx4 v16, %8, s55 offen offset:2048 lds\n"
"s_add_u32 s55, s54, 455680\n"
"buffer_load_dwordx4 v16, %8, s55 offen offset:3072 lds\n"
"s_mov_b32 s55, 0\n"
"buffer_load_dwordx2 v[32:33], v18, %10, s55 offen offset:0\n"
"buffer_load_dwordx2 v[34:35], v18, %10, s55 offen offset:1024\n"
"buffer_load_dwordx2 v[36:37], v18, %10, s55 offen offset:2048\n"
"buffer_load_dwordx2 v[38:39], v18, %10, s55 offen offset:3072\n"
"s_mov_b32 s55, 4096\n"
"buffer_load_dwordx2 v[40:41], v18, %10, s55 offen offset:0\n"
"buffer_load_dwordx2 v[42:43], v18, %10, s55 offen offset:1024\n"
"buffer_load_dwordx2 v[44:45], v18, %10, s55 offen offset:2048\n"
"buffer_load_dwordx2 v[46:47], v18, %10, s55 offen offset:3072\n"
"s_mov_b32 s55, 32768\n"
"buffer_load_dwordx2 v[48:49], v18, %10, s55 offen offset:0\n"
"buffer_load_dwordx2 v[50:51], v18, %10, s55 offen offset:1024\n"
"buffer_load_dwordx2 v[52:53], v18, %10, s55 offen offset:2048\n"
"buffer_load_dwordx2 v[54:55], v18, %10, s55 offen offset:3072\n"
"s_mov_b32 s55, 36864\n"
"buffer_load_dwordx2 v[56:57], v18, %10, s55 offen offset:0\n"
"buffer_load_dwordx2 v[58:59], v18, %10, s55 offen offset:1024\n"
"buffer_load_dwordx2 v[60:61], v18, %10, s55 offen offset:2048\n"
"buffer_load_dwordx2 v[62:63], v18, %10, s55 offen offset:3072\n"
"v_add_u32 v112, s60, v24\n"
"v_add_u32 v114, s61, v24\n"
"v_add_u32 v113, 256, v112\n"
"v_add_u32 v115, 256, v114\n"
"v_add_u32 v116, s60, v25\n"
"v_add_u32 v118, s61, v25\n"
"v_add_u32 v117, 128, v116\n"
"v_add_u32 v119, 128, v118\n"
"s_waitcnt vmcnt(48)\n"
"s_barrier\n"
"v_readfirstlane_b32 s38, v27\n"
"v_perm_b32 v64, v121, v120, s56\n"
"v_perm_b32 v65, v121, v120, s57\n"
"v_perm_b32 v66, v123, v122, s56\n"
"v_perm_b32 v67, v123, v122, s57\n"
"v_perm_b32 v68, v125, v124, s56\n"
"v_perm_b32 v69, v125, v124, s57\n"
"v_perm_b32 v70, v127, v126, s56\n"
"v_perm_b32 v71, v127, v126, s57\n"
"v_min_u32 v64, -2, v64\n"
"v_min_u32 v65, -2, v65\n"
"v_min_u32 v66, -2, v66\n"
"v_min_u32 v67, -2, v67\n"
"v_min_u32 v68, -2, v68\n"
"v_min_u32 v69, -2, v69\n"
"v_min_u32 v70, -2, v70\n"
"v_min_u32 v71, -2, v71\n"
"s_add_u32 s38, s38, 128\n"
"v_readfirstlane_b32 s55, v31\n"
"s_add_u32 s54, s38, 1\n"
"v_mov_b32 v26, s54\n"
"s_nop 3\n"
"buffer_store_dword v26, off, %7, s55 sc1\n"
"s_waitcnt vmcnt(41)\n"
"ds_read2st64_b64 v[80:83], v114 offset0:0 offset1:1\n"
"ds_read2st64_b64 v[84:87], v115 offset0:0 offset1:1\n"
"ds_read2st64_b64 v[88:91], v114 offset0:2 offset1:3\n"
"ds_read2st64_b64 v[92:95], v115 offset0:2 offset1:3\n"
"ds_read2st64_b64 v[96:99], v114 offset0:8 offset1:9\n"
"ds_read2st64_b64 v[100:103], v115 offset0:8 offset1:9\n"
"ds_read2st64_b64 v[104:107], v114 offset0:10 offset1:11\n"
"ds_read2st64_b64 v[108:111], v115 offset0:10 offset1:11\n"
"s_waitcnt lgkmcnt(0)\n"
"s_lshl_b32 s54, %11, 1\n"
"s_add_u32 s54, s54, 4\n"
"s_and_b32 s54, s54, 7\n"
"s_lshl_b32 s54, s54, 19\n"
"s_add_u32 m0, s61, 0\n"
"s_add_u32 s55, s54, 0\n"
"buffer_load_dwordx4 v16, %8, s55 offen offset:0 lds\n"
"s_add_u32 s55, s54, 64512\n"
"buffer_load_dwordx4 v16, %8, s55 offen offset:1024 lds\n"
"s_add_u32 s55, s54, 129024\n"
"buffer_load_dwordx4 v16, %8, s55 offen offset:2048 lds\n"
"s_add_u32 s55, s54, 193536\n"
"buffer_load_dwordx4 v16, %8, s55 offen offset:3072 lds\n"
"s_add_u32 m0, s61, 4096\n"
"s_add_u32 s55, s54, 262144\n"
"buffer_load_dwordx4 v16, %8, s55 offen offset:0 lds\n"
"s_add_u32 s55, s54, 326656\n"
"buffer_load_dwordx4 v16, %8, s55 offen offset:1024 lds\n"
"s_add_u32 s55, s54, 391168\n"
"buffer_load_dwordx4 v16, %8, s55 offen offset:2048 lds\n"
"s_add_u32 s55, s54, 455680\n"
"buffer_load_dwordx4 v16, %8, s55 offen offset:3072 lds\n"
"v_perm_b32 v72, v82, v80, s56\n"
"v_perm_b32 v73, v82, v80, s57\n"
"v_perm_b32 v74, v83, v81, s56\n"
"v_perm_b32 v75, v83, v81, s57\n"
"v_perm_b32 v76, v86, v84, s56\n"
"v_perm_b32 v77, v86, v84, s57\n"
"v_perm_b32 v78, v87, v85, s56\n"
"v_perm_b32 v79, v87, v85, s57\n"
"v_accvgpr_write_b32 a0, v72\n"
"v_accvgpr_write_b32 a8, v73\n"
"v_accvgpr_write_b32 a16, v74\n"
"v_accvgpr_write_b32 a24, v75\n"
"v_accvgpr_write_b32 a2, v76\n"
"v_accvgpr_write_b32 a10, v77\n"
"v_accvgpr_write_b32 a18, v78\n"
"v_accvgpr_write_b32 a26, v79\n"
"v_perm_b32 v72, v90, v88, s56\n"
"v_perm_b32 v73, v90, v88, s57\n"
"v_perm_b32 v74, v91, v89, s56\n"
"v_perm_b32 v75, v91, v89, s57\n"
"v_perm_b32 v76, v94, v92, s56\n"
"v_perm_b32 v77, v94, v92, s57\n"
"v_perm_b32 v78, v95, v93, s56\n"
"v_perm_b32 v79, v95, v93, s57\n"
"v_accvgpr_write_b32 a1, v72\n"
"v_accvgpr_write_b32 a9, v73\n"
"v_accvgpr_write_b32 a17, v74\n"
"v_accvgpr_write_b32 a25, v75\n"
"v_accvgpr_write_b32 a3, v76\n"
"v_accvgpr_write_b32 a11, v77\n"
"v_accvgpr_write_b32 a19, v78\n"
"v_accvgpr_write_b32 a27, v79\n"
"v_perm_b32 v72, v98, v96, s56\n"
"v_perm_b32 v73, v98, v96, s57\n"
"v_perm_b32 v74, v99, v97, s56\n"
"v_perm_b32 v75, v99, v97, s57\n"
"v_perm_b32 v76, v102, v100, s56\n"
"v_perm_b32 v77, v102, v100, s57\n"
"v_perm_b32 v78, v103, v101, s56\n"
"v_perm_b32 v79, v103, v101, s57\n"
"v_accvgpr_write_b32 a4, v72\n"
"v_accvgpr_write_b32 a12, v73\n"
"v_accvgpr_write_b32 a20, v74\n"
"v_accvgpr_write_b32 a28, v75\n"
"v_accvgpr_write_b32 a6, v76\n"
"v_accvgpr_write_b32 a14, v77\n"
"v_accvgpr_write_b32 a22, v78\n"
"v_accvgpr_write_b32 a30, v79\n"
"v_perm_b32 v72, v106, v104, s56\n"
"v_perm_b32 v73, v106, v104, s57\n"
"v_perm_b32 v74, v107, v105, s56\n"
"v_perm_b32 v75, v107, v105, s57\n"
"v_perm_b32 v76, v110, v108, s56\n"
"v_perm_b32 v77, v110, v108, s57\n"
"v_perm_b32 v78, v111, v109, s56\n"
"v_perm_b32 v79, v111, v109, s57\n"
"v_accvgpr_write_b32 a5, v72\n"
"v_accvgpr_write_b32 a13, v73\n"
"v_accvgpr_write_b32 a21, v74\n"
"v_accvgpr_write_b32 a29, v75\n"
"v_accvgpr_write_b32 a7, v76\n"
"v_accvgpr_write_b32 a15, v77\n"
"v_accvgpr_write_b32 a23, v78\n"
"v_accvgpr_write_b32 a31, v79\n"
"s_waitcnt vmcnt(41)\n"
"ds_read2st64_b64 v[80:83], v112 offset0:0 offset1:1\n"
"ds_read2st64_b64 v[84:87], v113 offset0:0 offset1:1\n"
"ds_read2st64_b64 v[88:91], v112 offset0:2 offset1:3\n"
"ds_read2st64_b64 v[92:95], v113 offset0:2 offset1:3\n"
"ds_read2st64_b64 v[96:99], v112 offset0:8 offset1:9\n"
"ds_read2st64_b64 v[100:103], v113 offset0:8 offset1:9\n"
"ds_read2st64_b64 v[104:107], v112 offset0:10 offset1:11\n"
"ds_read2st64_b64 v[108:111], v113 offset0:10 offset1:11\n"
"s_waitcnt lgkmcnt(0)\n"
"s_lshl_b32 s54, %11, 1\n"
"s_add_u32 s54, s54, 5\n"
"s_and_b32 s54, s54, 7\n"
"s_lshl_b32 s54, s54, 19\n"
"s_add_u32 m0, s60, 0\n"
"s_add_u32 s55, s54, 0\n"
"buffer_load_dwordx4 v16, %8, s55 offen offset:0 lds\n"
"s_add_u32 s55, s54, 64512\n"
"buffer_load_dwordx4 v16, %8, s55 offen offset:1024 lds\n"
"s_add_u32 s55, s54, 129024\n"
"buffer_load_dwordx4 v16, %8, s55 offen offset:2048 lds\n"
"s_add_u32 s55, s54, 193536\n"
"buffer_load_dwordx4 v16, %8, s55 offen offset:3072 lds\n"
"s_add_u32 m0, s60, 4096\n"
"s_add_u32 s55, s54, 262144\n"
"buffer_load_dwordx4 v16, %8, s55 offen offset:0 lds\n"
"s_add_u32 s55, s54, 326656\n"
"buffer_load_dwordx4 v16, %8, s55 offen offset:1024 lds\n"
"s_add_u32 s55, s54, 391168\n"
"buffer_load_dwordx4 v16, %8, s55 offen offset:2048 lds\n"
"s_add_u32 s55, s54, 455680\n"
"buffer_load_dwordx4 v16, %8, s55 offen offset:3072 lds\n"
"v_perm_b32 v72, v82, v80, s56\n"
"v_perm_b32 v73, v82, v80, s57\n"
"v_perm_b32 v74, v83, v81, s56\n"
"v_perm_b32 v75, v83, v81, s57\n"
"v_perm_b32 v76, v86, v84, s56\n"
"v_perm_b32 v77, v86, v84, s57\n"
"v_perm_b32 v78, v87, v85, s56\n"
"v_perm_b32 v79, v87, v85, s57\n"
"v_accvgpr_write_b32 a32, v72\n"
"v_accvgpr_write_b32 a40, v73\n"
"v_accvgpr_write_b32 a48, v74\n"
"v_accvgpr_write_b32 a56, v75\n"
"v_accvgpr_write_b32 a34, v76\n"
"v_accvgpr_write_b32 a42, v77\n"
"v_accvgpr_write_b32 a50, v78\n"
"v_accvgpr_write_b32 a58, v79\n"
"v_perm_b32 v72, v90, v88, s56\n"
"v_perm_b32 v73, v90, v88, s57\n"
"v_perm_b32 v74, v91, v89, s56\n"
"v_perm_b32 v75, v91, v89, s57\n"
"v_perm_b32 v76, v94, v92, s56\n"
"v_perm_b32 v77, v94, v92, s57\n"
"v_perm_b32 v78, v95, v93, s56\n"
"v_perm_b32 v79, v95, v93, s57\n"
"v_accvgpr_write_b32 a33, v72\n"
"v_accvgpr_write_b32 a41, v73\n"
"v_accvgpr_write_b32 a49, v74\n"
"v_accvgpr_write_b32 a57, v75\n"
"v_accvgpr_write_b32 a35, v76\n"
"v_accvgpr_write_b32 a43, v77\n"
"v_accvgpr_write_b32 a51, v78\n"
"v_accvgpr_write_b32 a59, v79\n"
"v_perm_b32 v72, v98, v96, s56\n"
"v_perm_b32 v73, v98, v96, s57\n"
"v_perm_b32 v74, v99, v97, s56\n"
"v_perm_b32 v75, v99, v97, s57\n"
"v_perm_b32 v76, v102, v100, s56\n"
"v_perm_b32 v77, v102, v100, s57\n"
"v_perm_b32 v78, v103, v101, s56\n"
"v_perm_b32 v79, v103, v101, s57\n"
"v_accvgpr_write_b32 a36, v72\n"
"v_accvgpr_write_b32 a44, v73\n"
"v_accvgpr_write_b32 a52, v74\n"
"v_accvgpr_write_b32 a60, v75\n"
"v_accvgpr_write_b32 a38, v76\n"
"v_accvgpr_write_b32 a46, v77\n"
"v_accvgpr_write_b32 a54, v78\n"
"v_accvgpr_write_b32 a62, v79\n"
"v_perm_b32 v72, v106, v104, s56\n"
"v_perm_b32 v73, v106, v104, s57\n"
"v_perm_b32 v74, v107, v105, s56\n"
"v_perm_b32 v75, v107, v105, s57\n"
"v_perm_b32 v76, v110, v108, s56\n"
"v_perm_b32 v77, v110, v108, s57\n"
"v_perm_b32 v78, v111, v109, s56\n"
"v_perm_b32 v79, v111, v109, s57\n"
"v_accvgpr_write_b32 a37, v72\n"
"v_accvgpr_write_b32 a45, v73\n"
"v_accvgpr_write_b32 a53, v74\n"
"v_accvgpr_write_b32 a61, v75\n"
"v_accvgpr_write_b32 a39, v76\n"
"v_accvgpr_write_b32 a47, v77\n"
"v_accvgpr_write_b32 a55, v78\n"
"v_accvgpr_write_b32 a63, v79\n"
"s_waitcnt vmcnt(41)\n"
"ds_read2st64_b64 v[80:83], v112 offset0:16 offset1:17\n"
"ds_read2st64_b64 v[84:87], v113 offset0:16 offset1:17\n"
"ds_read2st64_b64 v[88:91], v112 offset0:18 offset1:19\n"
"ds_read2st64_b64 v[92:95], v113 offset0:18 offset1:19\n"
"ds_read2st64_b64 v[96:99], v112 offset0:24 offset1:25\n"
"ds_read2st64_b64 v[100:103], v113 offset0:24 offset1:25\n"
"ds_read2st64_b64 v[104:107], v112 offset0:26 offset1:27\n"
"ds_read2st64_b64 v[108:111], v113 offset0:26 offset1:27\n"
"s_waitcnt lgkmcnt(0)\n"
"s_lshl_b32 s54, %11, 1\n"
"s_add_u32 s54, s54, 6\n"
"s_and_b32 s54, s54, 7\n"
"s_lshl_b32 s54, s54, 19\n"
"s_add_u32 m0, s60, 8192\n"
"s_add_u32 s55, s54, 0\n"
"buffer_load_dwordx4 v16, %8, s55 offen offset:0 lds\n"
"s_add_u32 s55, s54, 64512\n"
"buffer_load_dwordx4 v16, %8, s55 offen offset:1024 lds\n"
"s_add_u32 s55, s54, 129024\n"
"buffer_load_dwordx4 v16, %8, s55 offen offset:2048 lds\n"
"s_add_u32 s55, s54, 193536\n"
"buffer_load_dwordx4 v16, %8, s55 offen offset:3072 lds\n"
"s_add_u32 m0, s60, 12288\n"
"s_add_u32 s55, s54, 262144\n"
"buffer_load_dwordx4 v16, %8, s55 offen offset:0 lds\n"
"s_add_u32 s55, s54, 326656\n"
"buffer_load_dwordx4 v16, %8, s55 offen offset:1024 lds\n"
"s_add_u32 s55, s54, 391168\n"
"buffer_load_dwordx4 v16, %8, s55 offen offset:2048 lds\n"
"s_add_u32 s55, s54, 455680\n"
"buffer_load_dwordx4 v16, %8, s55 offen offset:3072 lds\n"
"v_perm_b32 v72, v82, v80, s56\n"
"v_perm_b32 v73, v82, v80, s57\n"
"v_perm_b32 v74, v83, v81, s56\n"
"v_perm_b32 v75, v83, v81, s57\n"
"v_perm_b32 v76, v86, v84, s56\n"
"v_perm_b32 v77, v86, v84, s57\n"
"v_perm_b32 v78, v87, v85, s56\n"
"v_perm_b32 v79, v87, v85, s57\n"
"v_accvgpr_write_b32 a64, v72\n"
"v_accvgpr_write_b32 a72, v73\n"
"v_accvgpr_write_b32 a80, v74\n"
"v_accvgpr_write_b32 a88, v75\n"
"v_accvgpr_write_b32 a66, v76\n"
"v_accvgpr_write_b32 a74, v77\n"
"v_accvgpr_write_b32 a82, v78\n"
"v_accvgpr_write_b32 a90, v79\n"
"v_perm_b32 v72, v90, v88, s56\n"
"v_perm_b32 v73, v90, v88, s57\n"
"v_perm_b32 v74, v91, v89, s56\n"
"v_perm_b32 v75, v91, v89, s57\n"
"v_perm_b32 v76, v94, v92, s56\n"
"v_perm_b32 v77, v94, v92, s57\n"
"v_perm_b32 v78, v95, v93, s56\n"
"v_perm_b32 v79, v95, v93, s57\n"
"v_accvgpr_write_b32 a65, v72\n"
"v_accvgpr_write_b32 a73, v73\n"
"v_accvgpr_write_b32 a81, v74\n"
"v_accvgpr_write_b32 a89, v75\n"
"v_accvgpr_write_b32 a67, v76\n"
"v_accvgpr_write_b32 a75, v77\n"
"v_accvgpr_write_b32 a83, v78\n"
"v_accvgpr_write_b32 a91, v79\n"
"v_perm_b32 v72, v98, v96, s56\n"
"v_perm_b32 v73, v98, v96, s57\n"
"v_perm_b32 v74, v99, v97, s56\n"
"v_perm_b32 v75, v99, v97, s57\n"
"v_perm_b32 v76, v102, v100, s56\n"
"v_perm_b32 v77, v102, v100, s57\n"
"v_perm_b32 v78, v103, v101, s56\n"
"v_perm_b32 v79, v103, v101, s57\n"
"v_accvgpr_write_b32 a68, v72\n"
"v_accvgpr_write_b32 a76, v73\n"
"v_accvgpr_write_b32 a84, v74\n"
"v_accvgpr_write_b32 a92, v75\n"
"v_accvgpr_write_b32 a70, v76\n"
"v_accvgpr_write_b32 a78, v77\n"
"v_accvgpr_write_b32 a86, v78\n"
"v_accvgpr_write_b32 a94, v79\n"
"v_perm_b32 v72, v106, v104, s56\n"
"v_perm_b32 v73, v106, v104, s57\n"
"v_perm_b32 v74, v107, v105, s56\n"
"v_perm_b32 v75, v107, v105, s57\n"
"v_perm_b32 v76, v110, v108, s56\n"
"v_perm_b32 v77, v110, v108, s57\n"
"v_perm_b32 v78, v111, v109, s56\n"
"v_perm_b32 v79, v111, v109, s57\n"
"v_accvgpr_write_b32 a69, v72\n"
"v_accvgpr_write_b32 a77, v73\n"
"v_accvgpr_write_b32 a85, v74\n"
"v_accvgpr_write_b32 a93, v75\n"
"v_accvgpr_write_b32 a71, v76\n"
"v_accvgpr_write_b32 a79, v77\n"
"v_accvgpr_write_b32 a87, v78\n"
"v_accvgpr_write_b32 a95, v79\n"
"s_waitcnt vmcnt(41)\n"
"ds_read2st64_b64 v[80:83], v112 offset0:32 offset1:33\n"
"ds_read2st64_b64 v[84:87], v113 offset0:32 offset1:33\n"
"ds_read2st64_b64 v[88:91], v112 offset0:34 offset1:35\n"
"ds_read2st64_b64 v[92:95], v113 offset0:34 offset1:35\n"
"ds_read2st64_b64 v[96:99], v112 offset0:40 offset1:41\n"
"ds_read2st64_b64 v[100:103], v113 offset0:40 offset1:41\n"
"ds_read2st64_b64 v[104:107], v112 offset0:42 offset1:43\n"
"ds_read2st64_b64 v[108:111], v113 offset0:42 offset1:43\n"
"s_waitcnt lgkmcnt(0)\n"
"s_lshl_b32 s54, %11, 1\n"
"s_add_u32 s54, s54, 7\n"
"s_and_b32 s54, s54, 7\n"
"s_lshl_b32 s54, s54, 19\n"
"s_add_u32 m0, s60, 16384\n"
"s_add_u32 s55, s54, 0\n"
"buffer_load_dwordx4 v16, %8, s55 offen offset:0 lds\n"
"s_add_u32 s55, s54, 64512\n"
"buffer_load_dwordx4 v16, %8, s55 offen offset:1024 lds\n"
"s_add_u32 s55, s54, 129024\n"
"buffer_load_dwordx4 v16, %8, s55 offen offset:2048 lds\n"
"s_add_u32 s55, s54, 193536\n"
"buffer_load_dwordx4 v16, %8, s55 offen offset:3072 lds\n"
"s_add_u32 m0, s60, 20480\n"
"s_add_u32 s55, s54, 262144\n"
"buffer_load_dwordx4 v16, %8, s55 offen offset:0 lds\n"
"s_add_u32 s55, s54, 326656\n"
"buffer_load_dwordx4 v16, %8, s55 offen offset:1024 lds\n"
"s_add_u32 s55, s54, 391168\n"
"buffer_load_dwordx4 v16, %8, s55 offen offset:2048 lds\n"
"s_add_u32 s55, s54, 455680\n"
"buffer_load_dwordx4 v16, %8, s55 offen offset:3072 lds\n"
"v_perm_b32 v72, v82, v80, s56\n"
"v_perm_b32 v73, v82, v80, s57\n"
"v_perm_b32 v74, v83, v81, s56\n"
"v_perm_b32 v75, v83, v81, s57\n"
"v_perm_b32 v76, v86, v84, s56\n"
"v_perm_b32 v77, v86, v84, s57\n"
"v_perm_b32 v78, v87, v85, s56\n"
"v_perm_b32 v79, v87, v85, s57\n"
"v_accvgpr_write_b32 a96, v72\n"
"v_accvgpr_write_b32 a104, v73\n"
"v_accvgpr_write_b32 a112, v74\n"
"v_accvgpr_write_b32 a120, v75\n"
"v_accvgpr_write_b32 a98, v76\n"
"v_accvgpr_write_b32 a106, v77\n"
"v_accvgpr_write_b32 a114, v78\n"
"v_accvgpr_write_b32 a122, v79\n"
"v_perm_b32 v72, v90, v88, s56\n"
"v_perm_b32 v73, v90, v88, s57\n"
"v_perm_b32 v74, v91, v89, s56\n"
"v_perm_b32 v75, v91, v89, s57\n"
"v_perm_b32 v76, v94, v92, s56\n"
"v_perm_b32 v77, v94, v92, s57\n"
"v_perm_b32 v78, v95, v93, s56\n"
"v_perm_b32 v79, v95, v93, s57\n"
"v_accvgpr_write_b32 a97, v72\n"
"v_accvgpr_write_b32 a105, v73\n"
"v_accvgpr_write_b32 a113, v74\n"
"v_accvgpr_write_b32 a121, v75\n"
"v_accvgpr_write_b32 a99, v76\n"
"v_accvgpr_write_b32 a107, v77\n"
"v_accvgpr_write_b32 a115, v78\n"
"v_accvgpr_write_b32 a123, v79\n"
"v_perm_b32 v72, v98, v96, s56\n"
"v_perm_b32 v73, v98, v96, s57\n"
"v_perm_b32 v74, v99, v97, s56\n"
"v_perm_b32 v75, v99, v97, s57\n"
"v_perm_b32 v76, v102, v100, s56\n"
"v_perm_b32 v77, v102, v100, s57\n"
"v_perm_b32 v78, v103, v101, s56\n"
"v_perm_b32 v79, v103, v101, s57\n"
"v_accvgpr_write_b32 a100, v72\n"
"v_accvgpr_write_b32 a108, v73\n"
"v_accvgpr_write_b32 a116, v74\n"
"v_accvgpr_write_b32 a124, v75\n"
"v_accvgpr_write_b32 a102, v76\n"
"v_accvgpr_write_b32 a110, v77\n"
"v_accvgpr_write_b32 a118, v78\n"
"v_accvgpr_write_b32 a126, v79\n"
"v_perm_b32 v72, v106, v104, s56\n"
"v_perm_b32 v73, v106, v104, s57\n"
"v_perm_b32 v74, v107, v105, s56\n"
"v_perm_b32 v75, v107, v105, s57\n"
"v_perm_b32 v76, v110, v108, s56\n"
"v_perm_b32 v77, v110, v108, s57\n"
"v_perm_b32 v78, v111, v109, s56\n"
"v_perm_b32 v79, v111, v109, s57\n"
"v_accvgpr_write_b32 a101, v72\n"
"v_accvgpr_write_b32 a109, v73\n"
"v_accvgpr_write_b32 a117, v74\n"
"v_accvgpr_write_b32 a125, v75\n"
"v_accvgpr_write_b32 a103, v76\n"
"v_accvgpr_write_b32 a111, v77\n"
"v_accvgpr_write_b32 a119, v78\n"
"v_accvgpr_write_b32 a127, v79\n"
"s_waitcnt vmcnt(24)\n"
"ds_read2st64_b64 v[80:83], v114 offset0:0 offset1:1\n"
"ds_read2st64_b64 v[84:87], v115 offset0:0 offset1:1\n"
"ds_read2st64_b64 v[88:91], v114 offset0:2 offset1:3\n"
"ds_read2st64_b64 v[92:95], v115 offset0:2 offset1:3\n"
"ds_read2st64_b64 v[96:99], v114 offset0:8 offset1:9\n"
"ds_read2st64_b64 v[100:103], v115 offset0:8 offset1:9\n"
"ds_read2st64_b64 v[104:107], v114 offset0:10 offset1:11\n"
"ds_read2st64_b64 v[108:111], v115 offset0:10 offset1:11\n"
"s_waitcnt lgkmcnt(0)\n"
"s_lshl_b32 s54, %11, 1\n"
"s_add_u32 s54, s54, 4\n"
"s_and_b32 s54, s54, 7\n"
"s_lshl_b32 s54, s54, 19\n"
"s_add_u32 m0, s61, 0\n"
"s_add_u32 s55, s54, 0\n"
"buffer_load_dwordx4 v17, %9, s55 offen offset:0 lds\n"
"s_add_u32 s55, s54, 130048\n"
"buffer_load_dwordx4 v17, %9, s55 offen offset:1024 lds\n"
"s_add_u32 s55, s54, 260096\n"
"buffer_load_dwordx4 v17, %9, s55 offen offset:2048 lds\n"
"s_add_u32 s55, s54, 390144\n"
"buffer_load_dwordx4 v17, %9, s55 offen offset:3072 lds\n"
"s_lshl_b32 s54, %11, 1\n"
"s_add_u32 s54, s54, 5\n"
"s_and_b32 s54, s54, 7\n"
"s_lshl_b32 s54, s54, 19\n"
"s_add_u32 m0, s61, 4096\n"
"s_add_u32 s55, s54, 0\n"
"buffer_load_dwordx4 v17, %9, s55 offen offset:0 lds\n"
"s_add_u32 s55, s54, 130048\n"
"buffer_load_dwordx4 v17, %9, s55 offen offset:1024 lds\n"
"s_add_u32 s55, s54, 260096\n"
"buffer_load_dwordx4 v17, %9, s55 offen offset:2048 lds\n"
"s_add_u32 s55, s54, 390144\n"
"buffer_load_dwordx4 v17, %9, s55 offen offset:3072 lds\n"
"s_mov_b32 s55, 65536\n"
"buffer_load_dwordx2 v[128:129], v18, %10, s55 offen offset:0\n"
"buffer_load_dwordx2 v[130:131], v18, %10, s55 offen offset:1024\n"
"buffer_load_dwordx2 v[132:133], v18, %10, s55 offen offset:2048\n"
"buffer_load_dwordx2 v[134:135], v18, %10, s55 offen offset:3072\n"
"s_mov_b32 s55, 69632\n"
"buffer_load_dwordx2 v[136:137], v18, %10, s55 offen offset:0\n"
"buffer_load_dwordx2 v[138:139], v18, %10, s55 offen offset:1024\n"
"buffer_load_dwordx2 v[140:141], v18, %10, s55 offen offset:2048\n"
"buffer_load_dwordx2 v[142:143], v18, %10, s55 offen offset:3072\n"
"s_mov_b32 s55, 98304\n"
"buffer_load_dwordx2 v[144:145], v18, %10, s55 offen offset:0\n"
"buffer_load_dwordx2 v[146:147], v18, %10, s55 offen offset:1024\n"
"buffer_load_dwordx2 v[148:149], v18, %10, s55 offen offset:2048\n"
"buffer_load_dwordx2 v[150:151], v18, %10, s55 offen offset:3072\n"
"s_mov_b32 s55, 102400\n"
"buffer_load_dwordx2 v[152:153], v18, %10, s55 offen offset:0\n"
"buffer_load_dwordx2 v[154:155], v18, %10, s55 offen offset:1024\n"
"buffer_load_dwordx2 v[156:157], v18, %10, s55 offen offset:2048\n"
"buffer_load_dwordx2 v[158:159], v18, %10, s55 offen offset:3072\n"
"v_perm_b32 v72, v82, v80, s56\n"
"v_perm_b32 v73, v82, v80, s57\n"
"v_perm_b32 v74, v83, v81, s56\n"
"v_perm_b32 v75, v83, v81, s57\n"
"v_perm_b32 v76, v86, v84, s56\n"
"v_perm_b32 v77, v86, v84, s57\n"
"v_perm_b32 v78, v87, v85, s56\n"
"v_perm_b32 v79, v87, v85, s57\n"
"v_accvgpr_write_b32 a128, v72\n"
"v_accvgpr_write_b32 a136, v73\n"
"v_accvgpr_write_b32 a144, v74\n"
"v_accvgpr_write_b32 a152, v75\n"
"v_accvgpr_write_b32 a130, v76\n"
"v_accvgpr_write_b32 a138, v77\n"
"v_accvgpr_write_b32 a146, v78\n"
"v_accvgpr_write_b32 a154, v79\n"
"v_perm_b32 v72, v90, v88, s56\n"
"v_perm_b32 v73, v90, v88, s57\n"
"v_perm_b32 v74, v91, v89, s56\n"
"v_perm_b32 v75, v91, v89, s57\n"
"v_perm_b32 v76, v94, v92, s56\n"
"v_perm_b32 v77, v94, v92, s57\n"
"v_perm_b32 v78, v95, v93, s56\n"
"v_perm_b32 v79, v95, v93, s57\n"
"v_accvgpr_write_b32 a129, v72\n"
"v_accvgpr_write_b32 a137, v73\n"
"v_accvgpr_write_b32 a145, v74\n"
"v_accvgpr_write_b32 a153, v75\n"
"v_accvgpr_write_b32 a131, v76\n"
"v_accvgpr_write_b32 a139, v77\n"
"v_accvgpr_write_b32 a147, v78\n"
"v_accvgpr_write_b32 a155, v79\n"
"v_perm_b32 v72, v98, v96, s56\n"
"v_perm_b32 v73, v98, v96, s57\n"
"v_perm_b32 v74, v99, v97, s56\n"
"v_perm_b32 v75, v99, v97, s57\n"
"v_perm_b32 v76, v102, v100, s56\n"
"v_perm_b32 v77, v102, v100, s57\n"
"v_perm_b32 v78, v103, v101, s56\n"
"v_perm_b32 v79, v103, v101, s57\n"
"v_accvgpr_write_b32 a132, v72\n"
"v_accvgpr_write_b32 a140, v73\n"
"v_accvgpr_write_b32 a148, v74\n"
"v_accvgpr_write_b32 a156, v75\n"
"v_accvgpr_write_b32 a134, v76\n"
"v_accvgpr_write_b32 a142, v77\n"
"v_accvgpr_write_b32 a150, v78\n"
"v_accvgpr_write_b32 a158, v79\n"
"v_perm_b32 v72, v106, v104, s56\n"
"v_perm_b32 v73, v106, v104, s57\n"
"v_perm_b32 v74, v107, v105, s56\n"
"v_perm_b32 v75, v107, v105, s57\n"
"v_perm_b32 v76, v110, v108, s56\n"
"v_perm_b32 v77, v110, v108, s57\n"
"v_perm_b32 v78, v111, v109, s56\n"
"v_perm_b32 v79, v111, v109, s57\n"
"v_accvgpr_write_b32 a133, v72\n"
"v_accvgpr_write_b32 a141, v73\n"
"v_accvgpr_write_b32 a149, v74\n"
"v_accvgpr_write_b32 a157, v75\n"
"v_accvgpr_write_b32 a135, v76\n"
"v_accvgpr_write_b32 a143, v77\n"
"v_accvgpr_write_b32 a151, v78\n"
"v_accvgpr_write_b32 a159, v79\n"
"s_waitcnt vmcnt(40)\n"
"ds_read2st64_b64 v[80:83], v112 offset0:0 offset1:1\n"
"ds_read2st64_b64 v[84:87], v113 offset0:0 offset1:1\n"
"ds_read2st64_b64 v[88:91], v112 offset0:2 offset1:3\n"
"ds_read2st64_b64 v[92:95], v113 offset0:2 offset1:3\n"
"ds_read2st64_b64 v[96:99], v112 offset0:8 offset1:9\n"
"ds_read2st64_b64 v[100:103], v113 offset0:8 offset1:9\n"
"ds_read2st64_b64 v[104:107], v112 offset0:10 offset1:11\n"
"ds_read2st64_b64 v[108:111], v113 offset0:10 offset1:11\n"
"s_waitcnt lgkmcnt(0)\n"
"s_lshl_b32 s54, %11, 1\n"
"s_add_u32 s54, s54, 0\n"
"s_and_b32 s54, s54, 7\n"
"s_lshl_b32 s54, s54, 19\n"
"s_add_u32 m0, s60, 0\n"
"s_add_u32 s55, s54, 0\n"
"buffer_load_dwordx4 v17, %9, s55 offen offset:0 lds\n"
"s_add_u32 s55, s54, 130048\n"
"buffer_load_dwordx4 v17, %9, s55 offen offset:1024 lds\n"
"s_add_u32 s55, s54, 260096\n"
"buffer_load_dwordx4 v17, %9, s55 offen offset:2048 lds\n"
"s_add_u32 s55, s54, 390144\n"
"buffer_load_dwordx4 v17, %9, s55 offen offset:3072 lds\n"
"s_lshl_b32 s54, %11, 1\n"
"s_add_u32 s54, s54, 1\n"
"s_and_b32 s54, s54, 7\n"
"s_lshl_b32 s54, s54, 19\n"
"s_add_u32 m0, s60, 4096\n"
"s_add_u32 s55, s54, 0\n"
"buffer_load_dwordx4 v17, %9, s55 offen offset:0 lds\n"
"s_add_u32 s55, s54, 130048\n"
"buffer_load_dwordx4 v17, %9, s55 offen offset:1024 lds\n"
"s_add_u32 s55, s54, 260096\n"
"buffer_load_dwordx4 v17, %9, s55 offen offset:2048 lds\n"
"s_add_u32 s55, s54, 390144\n"
"buffer_load_dwordx4 v17, %9, s55 offen offset:3072 lds\n"
"v_perm_b32 v72, v82, v80, s56\n"
"v_perm_b32 v73, v82, v80, s57\n"
"v_perm_b32 v74, v83, v81, s56\n"
"v_perm_b32 v75, v83, v81, s57\n"
"v_perm_b32 v76, v86, v84, s56\n"
"v_perm_b32 v77, v86, v84, s57\n"
"v_perm_b32 v78, v87, v85, s56\n"
"v_perm_b32 v79, v87, v85, s57\n"
"v_accvgpr_write_b32 a160, v72\n"
"v_accvgpr_write_b32 a168, v73\n"
"v_accvgpr_write_b32 a176, v74\n"
"v_accvgpr_write_b32 a184, v75\n"
"v_accvgpr_write_b32 a162, v76\n"
"v_accvgpr_write_b32 a170, v77\n"
"v_accvgpr_write_b32 a178, v78\n"
"v_accvgpr_write_b32 a186, v79\n"
"v_perm_b32 v72, v90, v88, s56\n"
"v_perm_b32 v73, v90, v88, s57\n"
"v_perm_b32 v74, v91, v89, s56\n"
"v_perm_b32 v75, v91, v89, s57\n"
"v_perm_b32 v76, v94, v92, s56\n"
"v_perm_b32 v77, v94, v92, s57\n"
"v_perm_b32 v78, v95, v93, s56\n"
"v_perm_b32 v79, v95, v93, s57\n"
"v_accvgpr_write_b32 a161, v72\n"
"v_accvgpr_write_b32 a169, v73\n"
"v_accvgpr_write_b32 a177, v74\n"
"v_accvgpr_write_b32 a185, v75\n"
"v_accvgpr_write_b32 a163, v76\n"
"v_accvgpr_write_b32 a171, v77\n"
"v_accvgpr_write_b32 a179, v78\n"
"v_accvgpr_write_b32 a187, v79\n"
"v_perm_b32 v72, v98, v96, s56\n"
"v_perm_b32 v73, v98, v96, s57\n"
"v_perm_b32 v74, v99, v97, s56\n"
"v_perm_b32 v75, v99, v97, s57\n"
"v_perm_b32 v76, v102, v100, s56\n"
"v_perm_b32 v77, v102, v100, s57\n"
"v_perm_b32 v78, v103, v101, s56\n"
"v_perm_b32 v79, v103, v101, s57\n"
"v_accvgpr_write_b32 a164, v72\n"
"v_accvgpr_write_b32 a172, v73\n"
"v_accvgpr_write_b32 a180, v74\n"
"v_accvgpr_write_b32 a188, v75\n"
"v_accvgpr_write_b32 a166, v76\n"
"v_accvgpr_write_b32 a174, v77\n"
"v_accvgpr_write_b32 a182, v78\n"
"v_accvgpr_write_b32 a190, v79\n"
"v_perm_b32 v72, v106, v104, s56\n"
"v_perm_b32 v73, v106, v104, s57\n"
"v_perm_b32 v74, v107, v105, s56\n"
"v_perm_b32 v75, v107, v105, s57\n"
"v_perm_b32 v76, v110, v108, s56\n"
"v_perm_b32 v77, v110, v108, s57\n"
"v_perm_b32 v78, v111, v109, s56\n"
"v_perm_b32 v79, v111, v109, s57\n"
"v_accvgpr_write_b32 a165, v72\n"
"v_accvgpr_write_b32 a173, v73\n"
"v_accvgpr_write_b32 a181, v74\n"
"v_accvgpr_write_b32 a189, v75\n"
"v_accvgpr_write_b32 a167, v76\n"
"v_accvgpr_write_b32 a175, v77\n"
"v_accvgpr_write_b32 a183, v78\n"
"v_accvgpr_write_b32 a191, v79\n"
"s_waitcnt vmcnt(40)\n"
"ds_read2st64_b64 v[80:83], v112 offset0:16 offset1:17\n"
"ds_read2st64_b64 v[84:87], v113 offset0:16 offset1:17\n"
"ds_read2st64_b64 v[88:91], v112 offset0:18 offset1:19\n"
"ds_read2st64_b64 v[92:95], v113 offset0:18 offset1:19\n"
"ds_read2st64_b64 v[96:99], v112 offset0:24 offset1:25\n"
"ds_read2st64_b64 v[100:103], v113 offset0:24 offset1:25\n"
"ds_read2st64_b64 v[104:107], v112 offset0:26 offset1:27\n"
"ds_read2st64_b64 v[108:111], v113 offset0:26 offset1:27\n"
"s_waitcnt lgkmcnt(0)\n"
"s_lshl_b32 s54, %11, 1\n"
"s_add_u32 s54, s54, 2\n"
"s_and_b32 s54, s54, 7\n"
"s_lshl_b32 s54, s54, 19\n"
"s_add_u32 m0, s60, 8192\n"
"s_add_u32 s55, s54, 0\n"
"buffer_load_dwordx4 v17, %9, s55 offen offset:0 lds\n"
"s_add_u32 s55, s54, 130048\n"
"buffer_load_dwordx4 v17, %9, s55 offen offset:1024 lds\n"
"s_add_u32 s55, s54, 260096\n"
"buffer_load_dwordx4 v17, %9, s55 offen offset:2048 lds\n"
"s_add_u32 s55, s54, 390144\n"
"buffer_load_dwordx4 v17, %9, s55 offen offset:3072 lds\n"
"s_lshl_b32 s54, %11, 1\n"
"s_add_u32 s54, s54, 3\n"
"s_and_b32 s54, s54, 7\n"
"s_lshl_b32 s54, s54, 19\n"
"s_add_u32 m0, s60, 12288\n"
"s_add_u32 s55, s54, 0\n"
"buffer_load_dwordx4 v17, %9, s55 offen offset:0 lds\n"
"s_add_u32 s55, s54, 130048\n"
"buffer_load_dwordx4 v17, %9, s55 offen offset:1024 lds\n"
"s_add_u32 s55, s54, 260096\n"
"buffer_load_dwordx4 v17, %9, s55 offen offset:2048 lds\n"
"s_add_u32 s55, s54, 390144\n"
"buffer_load_dwordx4 v17, %9, s55 offen offset:3072 lds\n"
"v_perm_b32 v72, v82, v80, s56\n"
"v_perm_b32 v73, v82, v80, s57\n"
"v_perm_b32 v74, v83, v81, s56\n"
"v_perm_b32 v75, v83, v81, s57\n"
"v_perm_b32 v76, v86, v84, s56\n"
"v_perm_b32 v77, v86, v84, s57\n"
"v_perm_b32 v78, v87, v85, s56\n"
"v_perm_b32 v79, v87, v85, s57\n"
"v_accvgpr_write_b32 a192, v72\n"
"v_accvgpr_write_b32 a200, v73\n"
"v_accvgpr_write_b32 a208, v74\n"
"v_accvgpr_write_b32 a216, v75\n"
"v_accvgpr_write_b32 a194, v76\n"
"v_accvgpr_write_b32 a202, v77\n"
"v_accvgpr_write_b32 a210, v78\n"
"v_accvgpr_write_b32 a218, v79\n"
"v_perm_b32 v72, v90, v88, s56\n"
"v_perm_b32 v73, v90, v88, s57\n"
"v_perm_b32 v74, v91, v89, s56\n"
"v_perm_b32 v75, v91, v89, s57\n"
"v_perm_b32 v76, v94, v92, s56\n"
"v_perm_b32 v77, v94, v92, s57\n"
"v_perm_b32 v78, v95, v93, s56\n"
"v_perm_b32 v79, v95, v93, s57\n"
"v_accvgpr_write_b32 a193, v72\n"
"v_accvgpr_write_b32 a201, v73\n"
"v_accvgpr_write_b32 a209, v74\n"
"v_accvgpr_write_b32 a217, v75\n"
"v_accvgpr_write_b32 a195, v76\n"
"v_accvgpr_write_b32 a203, v77\n"
"v_accvgpr_write_b32 a211, v78\n"
"v_accvgpr_write_b32 a219, v79\n"
"v_perm_b32 v72, v98, v96, s56\n"
"v_perm_b32 v73, v98, v96, s57\n"
"v_perm_b32 v74, v99, v97, s56\n"
"v_perm_b32 v75, v99, v97, s57\n"
"v_perm_b32 v76, v102, v100, s56\n"
"v_perm_b32 v77, v102, v100, s57\n"
"v_perm_b32 v78, v103, v101, s56\n"
"v_perm_b32 v79, v103, v101, s57\n"
"v_accvgpr_write_b32 a196, v72\n"
"v_accvgpr_write_b32 a204, v73\n"
"v_accvgpr_write_b32 a212, v74\n"
"v_accvgpr_write_b32 a220, v75\n"
"v_accvgpr_write_b32 a198, v76\n"
"v_accvgpr_write_b32 a206, v77\n"
"v_accvgpr_write_b32 a214, v78\n"
"v_accvgpr_write_b32 a222, v79\n"
"v_perm_b32 v72, v106, v104, s56\n"
"v_perm_b32 v73, v106, v104, s57\n"
"v_perm_b32 v74, v107, v105, s56\n"
"v_perm_b32 v75, v107, v105, s57\n"
"v_perm_b32 v76, v110, v108, s56\n"
"v_perm_b32 v77, v110, v108, s57\n"
"v_perm_b32 v78, v111, v109, s56\n"
"v_perm_b32 v79, v111, v109, s57\n"
"v_accvgpr_write_b32 a197, v72\n"
"v_accvgpr_write_b32 a205, v73\n"
"v_accvgpr_write_b32 a213, v74\n"
"v_accvgpr_write_b32 a221, v75\n"
"v_accvgpr_write_b32 a199, v76\n"
"v_accvgpr_write_b32 a207, v77\n"
"v_accvgpr_write_b32 a215, v78\n"
"v_accvgpr_write_b32 a223, v79\n"
"s_waitcnt vmcnt(40)\n"
"ds_read2st64_b64 v[80:83], v112 offset0:32 offset1:33\n"
"ds_read2st64_b64 v[84:87], v113 offset0:32 offset1:33\n"
"ds_read2st64_b64 v[88:91], v112 offset0:34 offset1:35\n"
"ds_read2st64_b64 v[92:95], v113 offset0:34 offset1:35\n"
"ds_read2st64_b64 v[96:99], v112 offset0:40 offset1:41\n"
"ds_read2st64_b64 v[100:103], v113 offset0:40 offset1:41\n"
"ds_read2st64_b64 v[104:107], v112 offset0:42 offset1:43\n"
"ds_read2st64_b64 v[108:111], v113 offset0:42 offset1:43\n"
"s_waitcnt lgkmcnt(0)\n"
"s_lshl_b32 s54, %11, 1\n"
"s_add_u32 s54, s54, 6\n"
"s_and_b32 s54, s54, 7\n"
"s_lshl_b32 s54, s54, 19\n"
"s_add_u32 m0, s60, 16384\n"
"s_add_u32 s55, s54, 0\n"
"buffer_load_dwordx4 v17, %9, s55 offen offset:0 lds\n"
"s_add_u32 s55, s54, 130048\n"
"buffer_load_dwordx4 v17, %9, s55 offen offset:1024 lds\n"
"s_add_u32 s55, s54, 260096\n"
"buffer_load_dwordx4 v17, %9, s55 offen offset:2048 lds\n"
"s_add_u32 s55, s54, 390144\n"
"buffer_load_dwordx4 v17, %9, s55 offen offset:3072 lds\n"
"s_lshl_b32 s54, %11, 1\n"
"s_add_u32 s54, s54, 7\n"
"s_and_b32 s54, s54, 7\n"
"s_lshl_b32 s54, s54, 19\n"
"s_add_u32 m0, s60, 20480\n"
"s_add_u32 s55, s54, 0\n"
"buffer_load_dwordx4 v17, %9, s55 offen offset:0 lds\n"
"s_add_u32 s55, s54, 130048\n"
"buffer_load_dwordx4 v17, %9, s55 offen offset:1024 lds\n"
"s_add_u32 s55, s54, 260096\n"
"buffer_load_dwordx4 v17, %9, s55 offen offset:2048 lds\n"
"s_add_u32 s55, s54, 390144\n"
"buffer_load_dwordx4 v17, %9, s55 offen offset:3072 lds\n"
"v_perm_b32 v72, v82, v80, s56\n"
"v_perm_b32 v73, v82, v80, s57\n"
"v_perm_b32 v74, v83, v81, s56\n"
"v_perm_b32 v75, v83, v81, s57\n"
"v_perm_b32 v76, v86, v84, s56\n"
"v_perm_b32 v77, v86, v84, s57\n"
"v_perm_b32 v78, v87, v85, s56\n"
"v_perm_b32 v79, v87, v85, s57\n"
"v_accvgpr_write_b32 a224, v72\n"
"v_accvgpr_write_b32 a232, v73\n"
"v_accvgpr_write_b32 a240, v74\n"
"v_accvgpr_write_b32 a248, v75\n"
"v_accvgpr_write_b32 a226, v76\n"
"v_accvgpr_write_b32 a234, v77\n"
"v_accvgpr_write_b32 a242, v78\n"
"v_accvgpr_write_b32 a250, v79\n"
"v_perm_b32 v72, v90, v88, s56\n"
"v_perm_b32 v73, v90, v88, s57\n"
"v_perm_b32 v74, v91, v89, s56\n"
"v_perm_b32 v75, v91, v89, s57\n"
"v_perm_b32 v76, v94, v92, s56\n"
"v_perm_b32 v77, v94, v92, s57\n"
"v_perm_b32 v78, v95, v93, s56\n"
"v_perm_b32 v79, v95, v93, s57\n"
"v_accvgpr_write_b32 a225, v72\n"
"v_accvgpr_write_b32 a233, v73\n"
"v_accvgpr_write_b32 a241, v74\n"
"v_accvgpr_write_b32 a249, v75\n"
"v_accvgpr_write_b32 a227, v76\n"
"v_accvgpr_write_b32 a235, v77\n"
"v_accvgpr_write_b32 a243, v78\n"
"v_accvgpr_write_b32 a251, v79\n"
"v_perm_b32 v72, v98, v96, s56\n"
"v_perm_b32 v73, v98, v96, s57\n"
"v_perm_b32 v74, v99, v97, s56\n"
"v_perm_b32 v75, v99, v97, s57\n"
"v_perm_b32 v76, v102, v100, s56\n"
"v_perm_b32 v77, v102, v100, s57\n"
"v_perm_b32 v78, v103, v101, s56\n"
"v_perm_b32 v79, v103, v101, s57\n"
"v_accvgpr_write_b32 a228, v72\n"
"v_accvgpr_write_b32 a236, v73\n"
"v_accvgpr_write_b32 a244, v74\n"
"v_accvgpr_write_b32 a252, v75\n"
"v_accvgpr_write_b32 a230, v76\n"
"v_accvgpr_write_b32 a238, v77\n"
"v_accvgpr_write_b32 a246, v78\n"
"v_accvgpr_write_b32 a254, v79\n"
"v_perm_b32 v72, v106, v104, s56\n"
"v_perm_b32 v73, v106, v104, s57\n"
"v_perm_b32 v74, v107, v105, s56\n"
"v_perm_b32 v75, v107, v105, s57\n"
"v_perm_b32 v76, v110, v108, s56\n"
"v_perm_b32 v77, v110, v108, s57\n"
"v_perm_b32 v78, v111, v109, s56\n"
"v_perm_b32 v79, v111, v109, s57\n"
"v_accvgpr_write_b32 a229, v72\n"
"v_accvgpr_write_b32 a237, v73\n"
"v_accvgpr_write_b32 a245, v74\n"
"v_accvgpr_write_b32 a253, v75\n"
"v_accvgpr_write_b32 a231, v76\n"
"v_accvgpr_write_b32 a239, v77\n"
"v_accvgpr_write_b32 a247, v78\n"
"v_accvgpr_write_b32 a255, v79\n"
"s_mov_b32 s49, 0\n"
"v_mbcnt_lo_u32_b32 v26, -1, 0\n"
"v_mbcnt_hi_u32_b32 v26, -1, v26\n"
"v_lshlrev_b32 v26, 2, v26\n"
"s_waitcnt vmcnt(44)\n"
"ds_read2st64_b32 v[80:81], v118 offset0:0 offset1:1\n"
"ds_read2st64_b32 v[82:83], v119 offset0:0 offset1:1\n"
"ds_read2st64_b32 v[84:85], v118 offset0:2 offset1:3\n"
"ds_read2st64_b32 v[86:87], v119 offset0:2 offset1:3\n"
"ds_read2st64_b32 v[88:89], v118 offset0:8 offset1:9\n"
"ds_read2st64_b32 v[90:91], v119 offset0:8 offset1:9\n"
"ds_read2st64_b32 v[92:93], v118 offset0:10 offset1:11\n"
"ds_read2st64_b32 v[94:95], v119 offset0:10 offset1:11\n"
"s_waitcnt lgkmcnt(7)\n"
"v_perm_b32 v160, v81, v80, s56\n"
"v_perm_b32 v168, v81, v80, s57\n"
"s_waitcnt lgkmcnt(6)\n"
"v_perm_b32 v162, v83, v82, s56\n"
"v_perm_b32 v170, v83, v82, s57\n"
"s_waitcnt lgkmcnt(5)\n"
"v_perm_b32 v161, v85, v84, s56\n"
"v_perm_b32 v169, v85, v84, s57\n"
"s_waitcnt lgkmcnt(4)\n"
"v_perm_b32 v163, v87, v86, s56\n"
"v_perm_b32 v171, v87, v86, s57\n"
"s_waitcnt lgkmcnt(3)\n"
"v_perm_b32 v164, v89, v88, s56\n"
"v_perm_b32 v172, v89, v88, s57\n"
"s_waitcnt lgkmcnt(2)\n"
"v_perm_b32 v166, v91, v90, s56\n"
"v_perm_b32 v174, v91, v90, s57\n"
"s_waitcnt lgkmcnt(1)\n"
"v_perm_b32 v165, v93, v92, s56\n"
"v_perm_b32 v173, v93, v92, s57\n"
"s_waitcnt lgkmcnt(0)\n"
"v_perm_b32 v167, v95, v94, s56\n"
"v_perm_b32 v175, v95, v94, s57\n"
"s_waitcnt vmcnt(40)\n"
"ds_read2st64_b32 v[80:81], v118 offset0:16 offset1:17\n"
"ds_read2st64_b32 v[82:83], v119 offset0:16 offset1:17\n"
"ds_read2st64_b32 v[84:85], v118 offset0:18 offset1:19\n"
"ds_read2st64_b32 v[86:87], v119 offset0:18 offset1:19\n"
"ds_read2st64_b32 v[88:89], v118 offset0:24 offset1:25\n"
"ds_read2st64_b32 v[90:91], v119 offset0:24 offset1:25\n"
"ds_read2st64_b32 v[92:93], v118 offset0:26 offset1:27\n"
"ds_read2st64_b32 v[94:95], v119 offset0:26 offset1:27\n"
"s_waitcnt lgkmcnt(7)\n"
"v_perm_b32 v176, v81, v80, s56\n"
"v_perm_b32 v184, v81, v80, s57\n"
"s_waitcnt lgkmcnt(6)\n"
"v_perm_b32 v178, v83, v82, s56\n"
"v_perm_b32 v186, v83, v82, s57\n"
"s_waitcnt lgkmcnt(5)\n"
"v_perm_b32 v177, v85, v84, s56\n"
"v_perm_b32 v185, v85, v84, s57\n"
"s_waitcnt lgkmcnt(4)\n"
"v_perm_b32 v179, v87, v86, s56\n"
"v_perm_b32 v187, v87, v86, s57\n"
"s_waitcnt lgkmcnt(3)\n"
"v_perm_b32 v180, v89, v88, s56\n"
"v_perm_b32 v188, v89, v88, s57\n"
"s_waitcnt lgkmcnt(2)\n"
"v_perm_b32 v182, v91, v90, s56\n"
"v_perm_b32 v190, v91, v90, s57\n"
"s_waitcnt lgkmcnt(1)\n"
"v_perm_b32 v181, v93, v92, s56\n"
"v_perm_b32 v189, v93, v92, s57\n"
"s_waitcnt lgkmcnt(0)\n"
"v_perm_b32 v183, v95, v94, s56\n"
"v_perm_b32 v191, v95, v94, s57\n"
"s_waitcnt lgkmcnt(0)\n"
"s_barrier\n"
"s_waitcnt vmcnt(20)\n"
"ds_read2st64_b32 v[80:81], v116 offset0:0 offset1:1\n"
"ds_read2st64_b32 v[82:83], v117 offset0:0 offset1:1\n"
"ds_read2st64_b32 v[84:85], v116 offset0:2 offset1:3\n"
"ds_read2st64_b32 v[86:87], v117 offset0:2 offset1:3\n"
"ds_read2st64_b32 v[88:89], v116 offset0:8 offset1:9\n"
"ds_read2st64_b32 v[90:91], v117 offset0:8 offset1:9\n"
"ds_read2st64_b32 v[92:93], v116 offset0:10 offset1:11\n"
"ds_read2st64_b32 v[94:95], v117 offset0:10 offset1:11\n"
"s_waitcnt lgkmcnt(7)\n"
"v_perm_b32 v224, v81, v80, s56\n"
"v_perm_b32 v232, v81, v80, s57\n"
"s_waitcnt lgkmcnt(6)\n"
"v_perm_b32 v226, v83, v82, s56\n"
"v_perm_b32 v234, v83, v82, s57\n"
"s_waitcnt lgkmcnt(5)\n"
"v_perm_b32 v225, v85, v84, s56\n"
"v_perm_b32 v233, v85, v84, s57\n"
"s_waitcnt lgkmcnt(4)\n"
"v_perm_b32 v227, v87, v86, s56\n"
"v_perm_b32 v235, v87, v86, s57\n"
"s_waitcnt lgkmcnt(3)\n"
"v_perm_b32 v228, v89, v88, s56\n"
"v_perm_b32 v236, v89, v88, s57\n"
"s_waitcnt lgkmcnt(2)\n"
"v_perm_b32 v230, v91, v90, s56\n"
"v_perm_b32 v238, v91, v90, s57\n"
"s_waitcnt lgkmcnt(1)\n"
"v_perm_b32 v229, v93, v92, s56\n"
"v_perm_b32 v237, v93, v92, s57\n"
"s_waitcnt lgkmcnt(0)\n"
"v_perm_b32 v231, v95, v94, s56\n"
"v_perm_b32 v239, v95, v94, s57\n"
"ds_write_b128 v2, v[224:227] offset:0\n"
"ds_write_b128 v2, v[228:231] offset:1024\n"
"ds_write_b128 v2, v[232:235] offset:2048\n"
"ds_write_b128 v2, v[236:239] offset:3072\n"
"s_waitcnt vmcnt(16)\n"
"ds_read2st64_b32 v[80:81], v116 offset0:16 offset1:17\n"
"ds_read2st64_b32 v[82:83], v117 offset0:16 offset1:17\n"
"ds_read2st64_b32 v[84:85], v116 offset0:18 offset1:19\n"
"ds_read2st64_b32 v[86:87], v117 offset0:18 offset1:19\n"
"ds_read2st64_b32 v[88:89], v116 offset0:24 offset1:25\n"
"ds_read2st64_b32 v[90:91], v117 offset0:24 offset1:25\n"
"ds_read2st64_b32 v[92:93], v116 offset0:26 offset1:27\n"
"ds_read2st64_b32 v[94:95], v117 offset0:26 offset1:27\n"
"s_waitcnt lgkmcnt(7)\n"
"v_perm_b32 v240, v81, v80, s56\n"
"v_perm_b32 v248, v81, v80, s57\n"
"s_waitcnt lgkmcnt(6)\n"
"v_perm_b32 v242, v83, v82, s56\n"
"v_perm_b32 v250, v83, v82, s57\n"
"s_waitcnt lgkmcnt(5)\n"
"v_perm_b32 v241, v85, v84, s56\n"
"v_perm_b32 v249, v85, v84, s57\n"
"s_waitcnt lgkmcnt(4)\n"
"v_perm_b32 v243, v87, v86, s56\n"
"v_perm_b32 v251, v87, v86, s57\n"
"s_waitcnt lgkmcnt(3)\n"
"v_perm_b32 v244, v89, v88, s56\n"
"v_perm_b32 v252, v89, v88, s57\n"
"s_waitcnt lgkmcnt(2)\n"
"v_perm_b32 v246, v91, v90, s56\n"
"v_perm_b32 v254, v91, v90, s57\n"
"s_waitcnt lgkmcnt(1)\n"
"v_perm_b32 v245, v93, v92, s56\n"
"v_perm_b32 v253, v93, v92, s57\n"
"s_waitcnt lgkmcnt(0)\n"
"v_perm_b32 v247, v95, v94, s56\n"
"v_perm_b32 v255, v95, v94, s57\n"
"ds_write_b128 v2, v[240:243] offset:4096\n"
"ds_write_b128 v2, v[244:247] offset:5120\n"
"ds_write_b128 v2, v[248:251] offset:6144\n"
"ds_write_b128 v2, v[252:255] offset:7168\n"
"buffer_load_dword v27, v26, %7, 0 offen sc1\n"
"buffer_load_dword v28, v26, %7, 0 offen offset:256 sc1\n"
"buffer_load_dword v24, v21, %7, 0 offen sc1\n"
"s_waitcnt vmcnt(15)\n"
"ds_read2st64_b32 v[80:81], v116 offset0:32 offset1:33\n"
"ds_read2st64_b32 v[82:83], v117 offset0:32 offset1:33\n"
"ds_read2st64_b32 v[84:85], v116 offset0:34 offset1:35\n"
"ds_read2st64_b32 v[86:87], v117 offset0:34 offset1:35\n"
"ds_read2st64_b32 v[88:89], v116 offset0:40 offset1:41\n"
"ds_read2st64_b32 v[90:91], v117 offset0:40 offset1:41\n"
"ds_read2st64_b32 v[92:93], v116 offset0:42 offset1:43\n"
"ds_read2st64_b32 v[94:95], v117 offset0:42 offset1:43\n"
"s_waitcnt lgkmcnt(7)\n"
"v_perm_b32 v192, v81, v80, s56\n"
"v_perm_b32 v200, v81, v80, s57\n"
"s_waitcnt lgkmcnt(6)\n"
"v_perm_b32 v194, v83, v82, s56\n"
"v_perm_b32 v202, v83, v82, s57\n"
"s_waitcnt lgkmcnt(5)\n"
"v_perm_b32 v193, v85, v84, s56\n"
"v_perm_b32 v201, v85, v84, s57\n"
"s_waitcnt lgkmcnt(4)\n"
"v_perm_b32 v195, v87, v86, s56\n"
"v_perm_b32 v203, v87, v86, s57\n"
"s_waitcnt lgkmcnt(3)\n"
"v_perm_b32 v196, v89, v88, s56\n"
"v_perm_b32 v204, v89, v88, s57\n"
"s_waitcnt lgkmcnt(2)\n"
"v_perm_b32 v198, v91, v90, s56\n"
"v_perm_b32 v206, v91, v90, s57\n"
"s_waitcnt lgkmcnt(1)\n"
"v_perm_b32 v197, v93, v92, s56\n"
"v_perm_b32 v205, v93, v92, s57\n"
"s_waitcnt lgkmcnt(0)\n"
"v_perm_b32 v199, v95, v94, s56\n"
"v_perm_b32 v207, v95, v94, s57\n"
"ds_write_b128 v2, v[192:195] offset:8192\n"
"ds_write_b128 v2, v[196:199] offset:9216\n"
"ds_write_b128 v2, v[200:203] offset:10240\n"
"ds_write_b128 v2, v[204:207] offset:11264\n"
"s_waitcnt vmcnt(11)\n"
"ds_read2st64_b32 v[80:81], v116 offset0:48 offset1:49\n"
"ds_read2st64_b32 v[82:83], v117 offset0:48 offset1:49\n"
"ds_read2st64_b32 v[84:85], v116 offset0:50 offset1:51\n"
"ds_read2st64_b32 v[86:87], v117 offset0:50 offset1:51\n"
"ds_read2st64_b32 v[88:89], v116 offset0:56 offset1:57\n"
"ds_read2st64_b32 v[90:91], v117 offset0:56 offset1:57\n"
"ds_read2st64_b32 v[92:93], v116 offset0:58 offset1:59\n"
"ds_read2st64_b32 v[94:95], v117 offset0:58 offset1:59\n"
"s_waitcnt lgkmcnt(7)\n"
"v_perm_b32 v208, v81, v80, s56\n"
"v_perm_b32 v216, v81, v80, s57\n"
"s_waitcnt lgkmcnt(6)\n"
"v_perm_b32 v210, v83, v82, s56\n"
"v_perm_b32 v218, v83, v82, s57\n"
"s_waitcnt lgkmcnt(5)\n"
"v_perm_b32 v209, v85, v84, s56\n"
"v_perm_b32 v217, v85, v84, s57\n"
"s_waitcnt lgkmcnt(4)\n"
"v_perm_b32 v211, v87, v86, s56\n"
"v_perm_b32 v219, v87, v86, s57\n"
"s_waitcnt lgkmcnt(3)\n"
"v_perm_b32 v212, v89, v88, s56\n"
"v_perm_b32 v220, v89, v88, s57\n"
"s_waitcnt lgkmcnt(2)\n"
"v_perm_b32 v214, v91, v90, s56\n"
"v_perm_b32 v222, v91, v90, s57\n"
"s_waitcnt lgkmcnt(1)\n"
"v_perm_b32 v213, v93, v92, s56\n"
"v_perm_b32 v221, v93, v92, s57\n"
"s_waitcnt lgkmcnt(0)\n"
"v_perm_b32 v215, v95, v94, s56\n"
"v_perm_b32 v223, v95, v94, s57\n"
"ds_write_b128 v2, v[208:211] offset:12288\n"
"ds_write_b128 v2, v[212:215] offset:13312\n"
"ds_write_b128 v2, v[216:219] offset:14336\n"
"ds_write_b128 v2, v[220:223] offset:15360\n"
"CH_RDY%=:\n"
"s_waitcnt vmcnt(0)\n"
"v_subrev_u32 v27, s38, v27\n"
"v_subrev_u32 v25, s38, v28\n"
"v_subrev_u32 v24, s38, v24\n"
"v_cmp_ne_u32 vcc, 1, v27\n"
"v_add_u32 v27, -1, v25\n"
"v_add_u32 v24, -1, v24\n"
"v_cmp_lt_u32 s[60:61], 1, v27\n"
"v_cmp_lt_u32 s[62:63], 62, v24\n"
"s_or_b32 s60, s60, s62\n"
"s_and_b32 s60, s60, 0xffff\n"
"s_or_b32 s60, s60, vcc_lo\n"
"s_or_b32 s60, s60, vcc_hi\n"
"s_cbranch_scc0 CH_RDY2%=\n"
"s_sleep 1\n"
"buffer_load_dword v27, v26, %7, 0 offen sc1\n"
"buffer_load_dword v28, v26, %7, 0 offen offset:256 sc1\n"
"buffer_load_dword v24, v21, %7, 0 offen sc1\n"
"s_add_u32 s49, s49, 1\n"
"s_cmp_gt_u32 s49, 0x10000\n"
"s_cbranch_scc0 CH_RDY%=\n"
"CH_RDY2%=:\n"
"v_cmp_eq_u32 vcc, 2, v25\n"
"s_and_b32 s60, vcc_lo, 0xffff\n"
"s_bcnt1_i32_b32 s60, s60\n"
"s_cmp_gt_u32 s60, 8\n"
"s_cselect_b32 s42, 8192, 0\n"
"v_readfirstlane_b32 s46, v20\n"
"v_readfirstlane_b32 s78, v23\n"
"v_readfirstlane_b32 s99, v22\n"
"s_xor_b32 s93, s93, s42\n"
"s_add_u32 s46, s46, s93\n"
"s_add_u32 s78, s78, s93\n"
"s_lshl_b32 s48, %11, 12\n"
"s_add_u32 s48, s48, 122880\n"
"s_lshl_b32 s59, s48, 1\n"
"s_add_u32 s59, s59, 16384\n"
"s_mov_b32 s94, 0\n"
"s_mov_b32 m0, s48\n"
"s_nop 0\n"
"buffer_load_dwordx4 v12, %7, s46 offen offset:0 sc1 lds\n"
"buffer_load_dwordx4 v12, %7, s46 offen offset:1024 sc1 lds\n"
"buffer_load_dwordx4 v12, %7, s46 offen offset:2048 sc1 lds\n"
"buffer_load_dwordx4 v12, %7, s46 offen offset:3072 sc1 lds\n"
"s_sub_u32 s48, s59, s48\n"
"s_add_u32 s46, s46, 32768\n"
"ds_read2st64_b32 v[80:81], v116 offset0:64 offset1:65\n"
"ds_read2st64_b32 v[82:83], v117 offset0:64 offset1:65\n"
"ds_read2st64_b32 v[84:85], v116 offset0:66 offset1:67\n"
"ds_read2st64_b32 v[86:87], v117 offset0:66 offset1:67\n"
"ds_read2st64_b32 v[88:89], v116 offset0:72 offset1:73\n"
"ds_read2st64_b32 v[90:91], v117 offset0:72 offset1:73\n"
"ds_read2st64_b32 v[92:93], v116 offset0:74 offset1:75\n"
"ds_read2st64_b32 v[94:95], v117 offset0:74 offset1:75\n"
"s_waitcnt lgkmcnt(7)\n"
"v_perm_b32 v224, v81, v80, s56\n"
"v_perm_b32 v232, v81, v80, s57\n"
"s_waitcnt lgkmcnt(6)\n"
"v_perm_b32 v226, v83, v82, s56\n"
"v_perm_b32 v234, v83, v82, s57\n"
"s_waitcnt lgkmcnt(5)\n"
"v_perm_b32 v225, v85, v84, s56\n"
"v_perm_b32 v233, v85, v84, s57\n"
"s_waitcnt lgkmcnt(4)\n"
"v_perm_b32 v227, v87, v86, s56\n"
"v_perm_b32 v235, v87, v86, s57\n"
"s_waitcnt lgkmcnt(3)\n"
"v_perm_b32 v228, v89, v88, s56\n"
"v_perm_b32 v236, v89, v88, s57\n"
"s_waitcnt lgkmcnt(2)\n"
"v_perm_b32 v230, v91, v90, s56\n"
"v_perm_b32 v238, v91, v90, s57\n"
"s_waitcnt lgkmcnt(1)\n"
"v_perm_b32 v229, v93, v92, s56\n"
"v_perm_b32 v237, v93, v92, s57\n"
"s_waitcnt lgkmcnt(0)\n"
"v_perm_b32 v231, v95, v94, s56\n"
"v_perm_b32 v239, v95, v94, s57\n"
"ds_write_b128 v2, v[224:227] offset:16384\n"
"ds_write_b128 v2, v[228:231] offset:17408\n"
"ds_write_b128 v2, v[232:235] offset:18432\n"
"ds_write_b128 v2, v[236:239] offset:19456\n"
"ds_read2st64_b32 v[80:81], v116 offset0:80 offset1:81\n"
"ds_read2st64_b32 v[82:83], v117 offset0:80 offset1:81\n"
"ds_read2st64_b32 v[84:85], v116 offset0:82 offset1:83\n"
"ds_read2st64_b32 v[86:87], v117 offset0:82 offset1:83\n"
"ds_read2st64_b32 v[88:89], v116 offset0:88 offset1:89\n"
"ds_read2st64_b32 v[90:91], v117 offset0:88 offset1:89\n"
"ds_read2st64_b32 v[92:93], v116 offset0:90 offset1:91\n"
"ds_read2st64_b32 v[94:95], v117 offset0:90 offset1:91\n"
"s_waitcnt lgkmcnt(7)\n"
"v_perm_b32 v240, v81, v80, s56\n"
"v_perm_b32 v248, v81, v80, s57\n"
"s_waitcnt lgkmcnt(6)\n"
"v_perm_b32 v242, v83, v82, s56\n"
"v_perm_b32 v250, v83, v82, s57\n"
"s_waitcnt lgkmcnt(5)\n"
"v_perm_b32 v241, v85, v84, s56\n"
"v_perm_b32 v249, v85, v84, s57\n"
"s_waitcnt lgkmcnt(4)\n"
"v_perm_b32 v243, v87, v86, s56\n"
"v_perm_b32 v251, v87, v86, s57\n"
"s_waitcnt lgkmcnt(3)\n"
"v_perm_b32 v244, v89, v88, s56\n"
"v_perm_b32 v252, v89, v88, s57\n"
"s_waitcnt lgkmcnt(2)\n"
"v_perm_b32 v246, v91, v90, s56\n"
"v_perm_b32 v254, v91, v90, s57\n"
"s_waitcnt lgkmcnt(1)\n"
"v_perm_b32 v245, v93, v92, s56\n"
"v_perm_b32 v253, v93, v92, s57\n"
"s_waitcnt lgkmcnt(0)\n"
"v_perm_b32 v247, v95, v94, s56\n"
"v_perm_b32 v255, v95, v94, s57\n"
"ds_write_b128 v2, v[240:243] offset:20480\n"
"ds_write_b128 v2, v[244:247] offset:21504\n"
"ds_write_b128 v2, v[248:251] offset:22528\n"
"ds_write_b128 v2, v[252:255] offset:23552\n"
"v_mov_b32 v112, v128\n"
"v_mov_b32 v72, v129\n"
"v_mov_b32 v113, v130\n"
"v_mov_b32 v73, v131\n"
"v_mov_b32 v114, v132\n"
"v_mov_b32 v74, v133\n"
"v_mov_b32 v115, v134\n"
"v_mov_b32 v75, v135\n"
"v_mov_b32 v116, v136\n"
"v_mov_b32 v76, v137\n"
"v_mov_b32 v117, v138\n"
"v_mov_b32 v77, v139\n"
"v_mov_b32 v118, v140\n"
"v_mov_b32 v78, v141\n"
"v_mov_b32 v119, v142\n"
"v_mov_b32 v79, v143\n"
"v_mov_b32 v120, v144\n"
"v_mov_b32 v80, v145\n"
"v_mov_b32 v121, v146\n"
"v_mov_b32 v81, v147\n"
"v_mov_b32 v122, v148\n"
"v_mov_b32 v82, v149\n"
"v_mov_b32 v123, v150\n"
"v_mov_b32 v83, v151\n"
"v_mov_b32 v124, v152\n"
"v_mov_b32 v84, v153\n"
"v_mov_b32 v125, v154\n"
"v_mov_b32 v85, v155\n"
"v_mov_b32 v126, v156\n"
"v_mov_b32 v86, v157\n"
"v_mov_b32 v127, v158\n"
"v_mov_b32 v87, v159\n"
"v_mov_b32 v144, v72\n"
"v_mov_b32 v145, v73\n"
"v_mov_b32 v146, v74\n"
"v_mov_b32 v147, v75\n"
"v_mov_b32 v148, v76\n"
"v_mov_b32 v149, v77\n"
"v_mov_b32 v150, v78\n"
"v_mov_b32 v151, v79\n"
"v_mov_b32 v152, v80\n"
"v_mov_b32 v153, v81\n"
"v_mov_b32 v154, v82\n"
"v_mov_b32 v155, v83\n"
"v_mov_b32 v156, v84\n"
"v_mov_b32 v157, v85\n"
"v_mov_b32 v158, v86\n"
"v_mov_b32 v159, v87\n"
"v_mov_b32 v96, v32\n"
"v_mov_b32 v128, v33\n"
"v_mov_b32 v97, v34\n"
"v_mov_b32 v129, v35\n"
"v_mov_b32 v98, v36\n"
"v_mov_b32 v130, v37\n"
"v_mov_b32 v99, v38\n"
"v_mov_b32 v131, v39\n"
"v_mov_b32 v100, v40\n"
"v_mov_b32 v132, v41\n"
"v_mov_b32 v101, v42\n"
"v_mov_b32 v133, v43\n"
"v_mov_b32 v102, v44\n"
"v_mov_b32 v134, v45\n"
"v_mov_b32 v103, v46\n"
"v_mov_b32 v135, v47\n"
"v_mov_b32 v104, v48\n"
"v_mov_b32 v136, v49\n"
"v_mov_b32 v105, v50\n"
"v_mov_b32 v137, v51\n"
"v_mov_b32 v106, v52\n"
"v_mov_b32 v138, v53\n"
"v_mov_b32 v107, v54\n"
"v_mov_b32 v139, v55\n"
"v_mov_b32 v108, v56\n"
"v_mov_b32 v140, v57\n"
"v_mov_b32 v109, v58\n"
"v_mov_b32 v141, v59\n"
"v_mov_b32 v110, v60\n"
"v_mov_b32 v142, v61\n"
"v_mov_b32 v111, v62\n"
"v_mov_b32 v143, v63\n"
"s_waitcnt vmcnt(0)\n"
"s_waitcnt lgkmcnt(0)\n"
"s_barrier\n"
"s_mov_b32 s40, 0\n"
"s_mov_b32 s44, s42\n"
"s_mov_b32 s47, 0\n"
"s_mov_b32 s92, 1280\n"
"s_mov_b32 s76, 80\n"
"s_lshr_b32 s41, s99, 7\n"
"s_and_b32 s41, s41, 63\n"
"s_cmp_eq_u32 %11, 2\n"
"s_cselect_b32 s98, s99, 0x40000000\n"
"s_mov_b32 s95, s38\n"
"s_add_u32 s53, s42, 0x10000\n"
"s_xor_b32 s45, s53, 0x20000\n"
"s_add_u32 s52, s53, 0x8000\n"
"s_add_u32 s77, s45, s37\n"
"s_add_u32 s79, s93, s42\n"
"s_add_u32 s93, s79, 0x80000\n"
"s_add_u32 s79, s79, s93\n"
"v_readfirstlane_b32 s62, v29\n"
"v_readfirstlane_b32 s63, v30\n"
"s_add_u32 s62, s62, s42\n"
"s_add_u32 s63, s63, s42\n"
"ds_read_b128 v[224:227], v9 offset:8192\n"
"ds_read_b128 v[240:243], v9 offset:12288\n"
"ds_read_b128 v[228:231], v9 offset:9216\n"
"ds_read_b128 v[244:247], v9 offset:13312\n"
"ds_read_b128 v[232:235], v9 offset:10240\n"
"ds_read_b128 v[248:251], v9 offset:14336\n"
"ds_read_b128 v[236:239], v9 offset:11264\n"
"ds_read_b128 v[252:255], v9 offset:15360\n"
"v_perm_b32 v48, v97, v96, s56\n"
"v_perm_b32 v49, v99, v98, s56\n"
"v_perm_b32 v50, v101, v100, s56\n"
"v_perm_b32 v51, v103, v102, s56\n"
"v_perm_b32 v52, v97, v96, s57\n"
"v_perm_b32 v53, v99, v98, s57\n"
"v_perm_b32 v54, v101, v100, s57\n"
"v_perm_b32 v55, v103, v102, s57\n"
"s_waitcnt lgkmcnt(7)\n"
"v_mfma_f32_16x16x32_bf16 v[192:195], v[224:227], v[48:51], 0\n"
"ds_read_b128 v[16:19], v9 offset:0\n"
"v_perm_b32 v56, v105, v104, s56\n"
"v_perm_b32 v57, v107, v106, s56\n"
"s_waitcnt lgkmcnt(7)\n"
"v_mfma_f32_16x16x32_bf16 v[200:203], v[240:243], v[48:51], 0\n"
"ds_read_b128 v[20:23], v9 offset:1024\n"
"v_perm_b32 v58, v109, v108, s56\n"
"v_perm_b32 v59, v111, v110, s56\n"
"v_mfma_f32_16x16x32_bf16 v[196:199], v[224:227], v[52:55], 0\n"
"ds_read_b128 v[24:27], v9 offset:2048\n"
"v_perm_b32 v60, v105, v104, s57\n"
"v_perm_b32 v61, v107, v106, s57\n"
"v_mfma_f32_16x16x32_bf16 v[204:207], v[240:243], v[52:55], 0\n"
"ds_read_b128 v[28:31], v9 offset:3072\n"
"v_perm_b32 v62, v109, v108, s57\n"
"v_perm_b32 v63, v111, v110, s57\n"
"s_waitcnt lgkmcnt(9)\n"
"v_mfma_f32_16x16x32_bf16 v[192:195], v[228:231], v[56:59], v[192:195]\n"
"ds_read_b128 v[32:35], v9 offset:4096\n"
"v_perm_b32 v48, v113, v112, s56\n"
"v_perm_b32 v49, v115, v114, s56\n"
"s_waitcnt lgkmcnt(9)\n"
"v_mfma_f32_16x16x32_bf16 v[200:203], v[244:247], v[56:59], v[200:203]\n"
"ds_read_b128 v[36:39], v9 offset:5120\n"
"v_perm_b32 v50, v117, v116, s56\n"
"v_perm_b32 v51, v119, v118, s56\n"
"v_mfma_f32_16x16x32_bf16 v[196:199], v[228:231], v[60:63], v[196:199]\n"
"v_perm_b32 v52, v113, v112, s57\n"
"v_perm_b32 v53, v115, v114, s57\n"
"v_mfma_f32_16x16x32_bf16 v[204:207], v[244:247], v[60:63], v[204:207]\n"
"v_perm_b32 v54, v117, v116, s57\n"
"v_perm_b32 v55, v119, v118, s57\n"
"s_waitcnt lgkmcnt(9)\n"
"v_mfma_f32_16x16x32_bf16 v[192:195], v[232:235], v[48:51], v[192:195]\n"
"v_perm_b32 v56, v121, v120, s56\n"
"v_perm_b32 v57, v123, v122, s56\n"
"s_waitcnt lgkmcnt(8)\n"
"v_mfma_f32_16x16x32_bf16 v[200:203], v[248:251], v[48:51], v[200:203]\n"
"v_perm_b32 v58, v125, v124, s56\n"
"v_perm_b32 v59, v127, v126, s56\n"
"v_mfma_f32_16x16x32_bf16 v[196:199], v[232:235], v[52:55], v[196:199]\n"
"v_perm_b32 v60, v121, v120, s57\n"
"v_perm_b32 v61, v123, v122, s57\n"
"v_mfma_f32_16x16x32_bf16 v[204:207], v[248:251], v[52:55], v[204:207]\n"
"v_perm_b32 v62, v125, v124, s57\n"
"v_perm_b32 v63, v127, v126, s57\n"
"s_waitcnt lgkmcnt(7)\n"
"v_mfma_f32_16x16x32_bf16 v[192:195], v[236:239], v[56:59], v[192:195]\n"
"v_perm_b32 v48, v129, v128, s56\n"
"v_perm_b32 v49, v131, v130, s56\n"
"v_mfma_f32_16x16x32_bf16 v[196:199], v[236:239], v[60:63], v[196:199]\n"
"v_perm_b32 v50, v133, v132, s56\n"
"v_perm_b32 v51, v135, v134, s56\n"
"s_waitcnt lgkmcnt(6)\n"
"v_mfma_f32_16x16x32_bf16 v[200:203], v[252:255], v[56:59], v[200:203]\n"
"v_perm_b32 v52, v129, v128, s57\n"
"v_perm_b32 v53, v131, v130, s57\n"
"v_mfma_f32_16x16x32_bf16 v[204:207], v[252:255], v[60:63], v[204:207]\n"
"v_perm_b32 v54, v133, v132, s57\n"
"v_perm_b32 v55, v135, v134, s57\n"
"v_cvt_pk_bf16_f32 v40, v192, v196\n"
"v_cvt_pk_bf16_f32 v41, v193, v197\n"
"v_cvt_pk_bf16_f32 v42, v194, v198\n"
"v_cvt_pk_bf16_f32 v43, v195, v199\n"
"s_nop 1\n"
"v_cvt_pk_bf16_f32 v44, v200, v204\n"
"v_cvt_pk_bf16_f32 v45, v201, v205\n"
"v_cvt_pk_bf16_f32 v46, v202, v206\n"
"v_cvt_pk_bf16_f32 v47, v203, v207\n"
"v_mfma_f32_16x16x32_bf16 v[192:195], v[224:227], v[48:51], 0\n"
"ds_write_b128 v2, v[40:43] offset:24576\n"
"v_perm_b32 v56, v137, v136, s56\n"
"v_perm_b32 v57, v139, v138, s56\n"
"v_mfma_f32_16x16x32_bf16 v[200:203], v[240:243], v[48:51], 0\n"
"ds_write_b128 v2, v[44:47] offset:25600\n"
"v_perm_b32 v58, v141, v140, s56\n"
"v_perm_b32 v59, v143, v142, s56\n"
"v_mfma_f32_16x16x32_bf16 v[196:199], v[224:227], v[52:55], 0\n"
"v_perm_b32 v60, v137, v136, s57\n"
"v_perm_b32 v61, v139, v138, s57\n"
"v_mfma_f32_16x16x32_bf16 v[204:207], v[240:243], v[52:55], 0\n"
"v_perm_b32 v62, v141, v140, s57\n"
"v_perm_b32 v63, v143, v142, s57\n"
"v_mfma_f32_16x16x32_bf16 v[192:195], v[228:231], v[56:59], v[192:195]\n"
"ds_read_b128 v[224:227], v2 offset:0\n"
"v_perm_b32 v48, v145, v144, s56\n"
"v_perm_b32 v49, v147, v146, s56\n"
"v_mfma_f32_16x16x32_bf16 v[200:203], v[244:247], v[56:59], v[200:203]\n"
"ds_read_b128 v[240:243], v2 offset:4096\n"
"v_perm_b32 v50, v149, v148, s56\n"
"v_perm_b32 v51, v151, v150, s56\n"
"v_mfma_f32_16x16x32_bf16 v[196:199], v[228:231], v[60:63], v[196:199]\n"
"v_perm_b32 v52, v145, v144, s57\n"
"v_perm_b32 v53, v147, v146, s57\n"
"v_mfma_f32_16x16x32_bf16 v[204:207], v[244:247], v[60:63], v[204:207]\n"
"v_perm_b32 v54, v149, v148, s57\n"
"v_perm_b32 v55, v151, v150, s57\n"
"v_mfma_f32_16x16x32_bf16 v[192:195], v[232:235], v[48:51], v[192:195]\n"
"ds_read_b128 v[228:231], v2 offset:1024\n"
"v_perm_b32 v56, v153, v152, s56\n"
"v_perm_b32 v57, v155, v154, s56\n"
"v_mfma_f32_16x16x32_bf16 v[200:203], v[248:251], v[48:51], v[200:203]\n"
"ds_read_b128 v[244:247], v2 offset:5120\n"
"v_perm_b32 v58, v157, v156, s56\n"
"v_perm_b32 v59, v159, v158, s56\n"
"v_mfma_f32_16x16x32_bf16 v[196:199], v[232:235], v[52:55], v[196:199]\n"
"v_perm_b32 v60, v153, v152, s57\n"
"v_perm_b32 v61, v155, v154, s57\n"
"v_mfma_f32_16x16x32_bf16 v[204:207], v[248:251], v[52:55], v[204:207]\n"
"v_perm_b32 v62, v157, v156, s57\n"
"v_perm_b32 v63, v159, v158, s57\n"
"v_mfma_f32_16x16x32_bf16 v[192:195], v[236:239], v[56:59], v[192:195]\n"
"ds_read_b128 v[232:235], v2 offset:2048\n"
"v_pk_mov_b32 v[48:49], 0, 0\n"
"v_mfma_f32_16x16x32_bf16 v[196:199], v[236:239], v[60:63], v[196:199]\n"
"ds_read_b128 v[248:251], v2 offset:6144\n"
"v_pk_mov_b32 v[50:51], 0, 0\n"
"v_mfma_f32_16x16x32_bf16 v[200:203], v[252:255], v[56:59], v[200:203]\n"
"v_pk_mov_b32 v[52:53], 0, 0\n"
"v_mfma_f32_16x16x32_bf16 v[204:207], v[252:255], v[60:63], v[204:207]\n"
"v_pk_mov_b32 v[54:55], 0, 0\n"
"ds_read_b128 v[236:239], v2 offset:3072\n"
"ds_read_b128 v[252:255], v2 offset:7168\n"
"v_pk_mov_b32 v[56:57], 0, 0\n"
"v_pk_mov_b32 v[58:59], 0, 0\n"
"v_pk_mov_b32 v[60:61], 0, 0\n"
"v_pk_mov_b32 v[62:63], 0, 0\n"
"v_cvt_pk_bf16_f32 v40, v192, v196\n"
"v_cvt_pk_bf16_f32 v41, v193, v197\n"
"v_cvt_pk_bf16_f32 v42, v194, v198\n"
"v_cvt_pk_bf16_f32 v43, v195, v199\n"
"v_cvt_pk_bf16_f32 v44, v200, v204\n"
"v_cvt_pk_bf16_f32 v45, v201, v205\n"
"v_cvt_pk_bf16_f32 v46, v202, v206\n"
"v_cvt_pk_bf16_f32 v47, v203, v207\n"
"ds_write_b128 v2, v[40:43] offset:26624\n"
"ds_write_b128 v2, v[44:47] offset:27648\n"
"ds_read_b128 v[192:195], v2 offset:8192\n"
"ds_read_b128 v[196:199], v2 offset:9216\n"
"ds_read_b128 v[200:203], v2 offset:10240\n"
"ds_read_b128 v[204:207], v2 offset:11264\n"
"v_pk_mov_b32 v[40:41], 0, 0\n"
"v_pk_mov_b32 v[42:43], 0, 0\n"
"v_pk_mov_b32 v[44:45], 0, 0\n"
"v_pk_mov_b32 v[46:47], 0, 0\n"
"s_waitcnt lgkmcnt(0)\n"
"s_nop 7\n"
".p2align 6\n"
"CH_LOOP%=:\n"
"s_waitcnt vmcnt(0)\n"
"ds_write_b128 v2, v[64:67] offset:28672\n"
"v_smfmac_f32_16x16x64_bf16 v[48:51], v[64:67], a[0:7], v0\n"
"ds_write_b128 v2, v[68:71] offset:29696\n"
"v_max3_u32 v72, v64, v65, v66\n"
"v_smfmac_f32_16x16x64_bf16 v[52:55], v[64:67], a[8:15], v0\n"
"v_max3_u32 v72, v72, v67, v68\n"
"v_smfmac_f32_16x16x64_bf16 v[56:59], v[64:67], a[16:23], v0\n"
"v_max3_u32 v72, v72, v69, v70\n"
"v_smfmac_f32_16x16x64_bf16 v[60:63], v[64:67], a[24:31], v0\n"
"v_max_u32 v72, v72, v71\n"
"v_cmp_eq_u32 vcc, -1, v72\n"
"v_smfmac_f32_16x16x64_bf16 v[48:51], v[68:71], a[32:39], v0\n"
"s_mov_b32 m0, s48\n"
"s_cbranch_vccnz CH_RETRY%=\n"
"s_waitcnt lgkmcnt(0)\n"
"s_barrier\n"
"ds_read_b128 v[72:75], v3 offset:0\n"
"ds_read_b128 v[76:79], v3 offset:1024\n"
"v_smfmac_f32_16x16x64_bf16 v[52:55], v[68:71], a[40:47], v0\n"
"ds_read_b128 v[80:83], v4 offset:0\n"
"s_memrealtime s[60:61]\n"
"v_smfmac_f32_16x16x64_bf16 v[56:59], v[68:71], a[48:55], v0\n"
"ds_read_b128 v[84:87], v4 offset:1024\n"
"v_smfmac_f32_16x16x64_bf16 v[60:63], v[68:71], a[56:63], v0\n"
"ds_read_b128 v[88:91], v5 offset:0\n"
"v_smfmac_f32_16x16x64_bf16 v[40:43], v[64:67], v[224:231], v0\n"
"ds_read_b128 v[92:95], v5 offset:1024\n"
"v_smfmac_f32_16x16x64_bf16 v[44:47], v[64:67], v[232:239], v0\n"
"buffer_load_dwordx4 v12, %7, s46 offen offset:0 sc1 lds\n"
"v_smfmac_f32_16x16x64_bf16 v[40:43], v[68:71], v[240:247], v0\n"
"buffer_load_dwordx4 v12, %7, s46 offen offset:1024 sc1 lds\n"
"v_smfmac_f32_16x16x64_bf16 v[44:47], v[68:71], v[248:255], v0\n"
"buffer_load_dwordx4 v12, %7, s46 offen offset:2048 sc1 lds\n"
"s_waitcnt lgkmcnt(0)\n"
"s_add_u32 s61, s60, s76\n"
"v_smfmac_f32_16x16x64_bf16 v[48:51], v[72:75], a[64:71], v0\n"
"buffer_load_dwordx4 v12, %7, s46 offen offset:3072 sc1 lds\n"
"v_mov_b32 v66, s61\n"
"v_smfmac_f32_16x16x64_bf16 v[48:51], v[76:79], a[96:103], v0\n"
"buffer_store_dword v66, v13, %7, s62 offen sc1\n"
"v_smfmac_f32_16x16x64_bf16 v[48:51], v[80:83], a[128:135], v0\n"
"ds_read_b128 v[228:231], v2 offset:17408\n"
"v_smfmac_f32_16x16x64_bf16 v[48:51], v[84:87], a[160:167], v0\n"
"ds_read_b128 v[232:235], v2 offset:18432\n"
"v_smfmac_f32_16x16x64_bf16 v[48:51], v[88:91], a[192:199], v0\n"
"ds_read_b128 v[236:239], v2 offset:19456\n"
"v_smfmac_f32_16x16x64_bf16 v[48:51], v[92:95], a[224:231], v0\n"
"ds_read_b128 v[240:243], v2 offset:20480\n"
"v_smfmac_f32_16x16x64_bf16 v[52:55], v[72:75], a[72:79], v0\n"
"s_mov_b32 m0, 162560\n"
"v_lshlrev_b32 v64, 2, v12\n"
"v_smfmac_f32_16x16x64_bf16 v[52:55], v[76:79], a[104:111], v0\n"
"ds_read_b128 v[224:227], v2 offset:16384\n"
"v_smfmac_f32_16x16x64_bf16 v[52:55], v[80:83], a[136:143], v0\n"
"v_smfmac_f32_16x16x64_bf16 v[52:55], v[84:87], a[168:175], v0\n"
"buffer_load_dword v64, %7, s78 offen sc1 lds\n"
"v_mov_b32 v65, s95\n"
"v_smfmac_f32_16x16x64_bf16 v[52:55], v[88:91], a[200:207], v0\n"
"ds_read_b128 v[244:247], v2 offset:21504\n"
"v_smfmac_f32_16x16x64_bf16 v[52:55], v[92:95], a[232:239], v0\n"
"buffer_store_dword v65, off, %7, s98 sc1\n"
"v_permlane32_swap_b32 v48, v50\n"
"v_smfmac_f32_16x16x64_bf16 v[56:59], v[72:75], a[80:87], v0\n"
"ds_read_b128 v[248:251], v2 offset:22528\n"
"v_permlane32_swap_b32 v49, v51\n"
"v_smfmac_f32_16x16x64_bf16 v[56:59], v[76:79], a[112:119], v0\n"
"ds_read_b128 v[252:255], v2 offset:23552\n"
"v_add_f32 v48, v48, v50\n"
"v_smfmac_f32_16x16x64_bf16 v[56:59], v[80:83], a[144:151], v0\n"
"s_nop 0\n"
"v_add_f32 v49, v49, v51\n"
"v_smfmac_f32_16x16x64_bf16 v[56:59], v[84:87], a[176:183], v0\n"
"ds_read_b128 v[208:211], v2 offset:12288\n"
"v_permlane32_swap_b32 v52, v54\n"
"v_smfmac_f32_16x16x64_bf16 v[56:59], v[88:91], a[208:215], v0\n"
"ds_read_b128 v[212:215], v2 offset:13312\n"
"v_permlane32_swap_b32 v53, v55\n"
"v_smfmac_f32_16x16x64_bf16 v[56:59], v[92:95], a[240:247], v0\n"
"ds_read_b128 v[216:219], v2 offset:14336\n"
"v_add_f32 v52, v52, v54\n"
"v_smfmac_f32_16x16x64_bf16 v[60:63], v[72:75], a[88:95], v0\n"
"s_nop 0\n"
"v_add_f32 v53, v53, v55\n"
"v_smfmac_f32_16x16x64_bf16 v[60:63], v[76:79], a[120:127], v0\n"
"ds_read_b128 v[220:223], v2 offset:15360\n"
"v_cvt_pk_bf16_f32 v50, v48, v52\n"
"v_smfmac_f32_16x16x64_bf16 v[60:63], v[80:83], a[152:159], v0\n"
"s_nop 0\n"
"v_cvt_pk_bf16_f32 v54, v49, v53\n"
"v_smfmac_f32_16x16x64_bf16 v[60:63], v[84:87], a[184:191], v0\n"
"ds_read_b128 v[64:67], v9 offset:6144\n"
"v_permlane32_swap_b32 v56, v58\n"
"v_smfmac_f32_16x16x64_bf16 v[60:63], v[88:91], a[216:223], v0\n"
"ds_read_b128 v[68:71], v9 offset:7168\n"
"v_permlane32_swap_b32 v57, v59\n"
"v_smfmac_f32_16x16x64_bf16 v[60:63], v[92:95], a[248:255], v0\n"
"ds_write2_b32 v6, v50, v54 offset1:72\n"
"s_nop 0\n"
"v_add_f32 v56, v56, v58\n"
"v_smfmac_f32_16x16x64_bf16 v[40:43], v[72:75], v[192:199], v0\n"
"s_nop 0\n"
"s_nop 0\n"
"v_add_f32 v57, v57, v59\n"
"v_xor_b32 v9, 0x3c000, v9\n"
"v_permlane32_swap_b32 v60, v62\n"
"v_permlane32_swap_b32 v61, v63\n"
"v_add_f32 v60, v60, v62\n"
"v_add_f32 v61, v61, v63\n"
"v_cvt_pk_bf16_f32 v51, v56, v60\n"
"v_cvt_pk_bf16_f32 v55, v57, v61\n"
"v_smfmac_f32_16x16x64_bf16 v[40:43], v[80:83], v[160:167], v0\n"
"ds_write2_b32 v6, v51, v55 offset0:1 offset1:73\n"
"v_smfmac_f32_16x16x64_bf16 v[40:43], v[84:87], v[176:183], v0\n"
"s_waitcnt lgkmcnt(4)\n"
"v_pk_mov_b32 v[192:193], 0, 0\n"
"v_smfmac_f32_16x16x64_bf16 v[40:43], v[88:91], v[224:231], v0\n"
"v_pk_mov_b32 v[194:195], 0, 0\n"
"v_smfmac_f32_16x16x64_bf16 v[40:43], v[92:95], v[240:247], v0\n"
"v_pk_mov_b32 v[196:197], 0, 0\n"
"v_smfmac_f32_16x16x64_bf16 v[40:43], v[76:79], v[208:215], v0\n"
"s_waitcnt lgkmcnt(0)\n"
"s_barrier\n"
"v_smfmac_f32_16x16x64_bf16 v[44:47], v[72:75], v[200:207], v0\n"
"ds_read_b128 v[48:51], v7 offset:0\n"
"v_smfmac_f32_16x16x64_bf16 v[44:47], v[76:79], v[216:223], v0\n"
"ds_read_b128 v[52:55], v7 offset:64\n"
"v_smfmac_f32_16x16x64_bf16 v[44:47], v[80:83], v[168:175], v0\n"
"ds_read_b128 v[56:59], v7 offset:128\n"
"v_permlane32_swap_b32 v40, v42\n"
"v_smfmac_f32_16x16x64_bf16 v[44:47], v[84:87], v[184:191], v0\n"
"ds_read_b128 v[60:63], v7 offset:192\n"
"v_permlane32_swap_b32 v41, v43\n"
"v_smfmac_f32_16x16x64_bf16 v[44:47], v[88:91], v[232:239], v0\n"
"ds_read_b128 v[72:75], v7 offset:2304\n"
"v_add_f32 v40, v40, v42\n"
"v_smfmac_f32_16x16x64_bf16 v[44:47], v[92:95], v[248:255], v0\n"
"s_waitcnt lgkmcnt(2)\n"
"v_add_f32 v41, v41, v43\n"
"v_mfma_f32_16x16x32_bf16 v[200:203], v[16:19], v[48:51], 0\n"
"ds_read_b128 v[76:79], v7 offset:2368\n"
"v_pk_mov_b32 v[198:199], 0, 0\n"
"v_mfma_f32_16x16x32_bf16 v[200:203], v[20:23], v[52:55], v[200:203]\n"
"ds_read_b128 v[80:83], v7 offset:2432\n"
"v_pk_mov_b32 v[208:209], 0, 0\n"
"v_mfma_f32_16x16x32_bf16 v[200:203], v[24:27], v[56:59], v[200:203]\n"
"s_waitcnt lgkmcnt(3)\n"
"v_permlane32_swap_b32 v44, v46\n"
"v_mfma_f32_16x16x32_bf16 v[200:203], v[28:31], v[60:63], v[200:203]\n"
"v_permlane32_swap_b32 v45, v47\n"
"v_mfma_f32_16x16x32_bf16 v[204:207], v[32:35], v[48:51], 0\n"
"v_add_f32 v44, v44, v46\n"
"v_add_f32 v45, v45, v47\n"
"v_mfma_f32_16x16x32_bf16 v[204:207], v[36:39], v[52:55], v[204:207]\n"
"v_cvt_pk_bf16_f32 v42, v40, v44\n"
"v_cvt_pk_bf16_f32 v43, v41, v45\n"
"v_mfma_f32_16x16x32_bf16 v[204:207], v[64:67], v[56:59], v[204:207]\n"
"ds_write2_b32 v8, v42, v43 offset1:72\n"
"v_mfma_f32_16x16x32_bf16 v[204:207], v[68:71], v[60:63], v[204:207]\n"
"ds_read_b128 v[84:87], v7 offset:2496\n"
"v_max3_f32 v92, v200, v201, v202\n"
"s_waitcnt lgkmcnt(4)\n"
"v_mfma_f32_16x16x32_bf16 v[216:219], v[72:75], v[48:51], 0\n"
"s_waitcnt lgkmcnt(2)\n"
"v_pk_mov_b32 v[210:211], 0, 0\n"
"v_pk_mov_b32 v[212:213], 0, 0\n"
"v_mfma_f32_16x16x32_bf16 v[216:219], v[76:79], v[52:55], v[216:219]\n"
"s_cmp_eq_u32 s40, 63\n"
"v_pk_mov_b32 v[214:215], 0, 0\n"
"v_pk_mov_b32 v[88:89], s[96:97], s[96:97]\n"
"v_mfma_f32_16x16x32_bf16 v[216:219], v[80:83], v[56:59], v[216:219]\n"
"s_waitcnt vmcnt(3) lgkmcnt(0)\n"
"s_barrier\n"
"v_mfma_f32_16x16x32_bf16 v[216:219], v[84:87], v[60:63], v[216:219]\n"
"ds_read_b128 v[224:227], v7 offset:4608\n"
"ds_read_b128 v[228:231], v7 offset:4672\n"
"v_max3_f32 v92, v92, v203, v204\n"
"v_max3_f32 v92, v92, v205, v206\n"
"v_pk_mov_b32 v[90:91], s[96:97], s[96:97]\n"
"s_cbranch_scc1 CH_END%=\n"
"v_max_f32 v92, v92, v207\n"
"v_mov_b32 v94, 0\n"
"v_max3_f32 v94, v216, v217, v218\n"
"v_max3_f32 v93, v92, v94, v219\n"
"v_max3_f32 v92, v92, v94, v219\n"
"s_nop 1\n"
"v_permlane16_swap_b32 v92, v93\n"
"v_max_f32 v94, v92, v93\n"
"v_max_f32 v92, v92, v93\n"
"s_waitcnt lgkmcnt(1)\n"
"v_smfmac_f32_16x16x64_bf16 v[192:195], v[224:227], v[96:103], v1\n"
"v_permlane32_swap_b32 v92, v94\n"
"v_max_f32 v92, v92, v94\n"
"v_mul_f32 v92, s58, v92\n"
"v_pk_fma_f32 v[200:201], v[200:201], s[58:59], v[92:93] op_sel_hi:[1,0,0] neg_lo:[0,0,1] neg_hi:[0,0,1]\n"
"v_pk_fma_f32 v[202:203], v[202:203], s[58:59], v[92:93] op_sel_hi:[1,0,0] neg_lo:[0,0,1] neg_hi:[0,0,1]\n"
"v_pk_fma_f32 v[204:205], v[204:205], s[58:59], v[92:93] op_sel_hi:[1,0,0] neg_lo:[0,0,1] neg_hi:[0,0,1]\n"
"v_pk_fma_f32 v[206:207], v[206:207], s[58:59], v[92:93] op_sel_hi:[1,0,0] neg_lo:[0,0,1] neg_hi:[0,0,1]\n"
"v_exp_f32 v200, v200\n"
"v_smfmac_f32_16x16x64_bf16 v[196:199], v[224:227], v[128:135], v1\n"
"ds_read_b128 v[232:235], v7 offset:4736\n"
"s_waitcnt lgkmcnt(1)\n"
"v_exp_f32 v201, v201\n"
"v_smfmac_f32_16x16x64_bf16 v[192:195], v[228:231], v[104:111], v1\n"
"ds_read_b128 v[236:239], v7 offset:4800\n"
"v_exp_f32 v202, v202\n"
"v_smfmac_f32_16x16x64_bf16 v[196:199], v[228:231], v[136:143], v1\n"
"ds_read_b128 v[240:243], v2 offset:24576\n"
"s_waitcnt lgkmcnt(2)\n"
"v_exp_f32 v203, v203\n"
"v_smfmac_f32_16x16x64_bf16 v[192:195], v[232:235], v[112:119], v1\n"
"ds_read_b128 v[244:247], v2 offset:25600\n"
"v_exp_f32 v204, v204\n"
"v_smfmac_f32_16x16x64_bf16 v[196:199], v[232:235], v[144:151], v1\n"
"buffer_load_dword v95, v12, %7, s63 offen sc1\n"
"s_waitcnt lgkmcnt(2)\n"
"v_exp_f32 v205, v205\n"
"v_smfmac_f32_16x16x64_bf16 v[192:195], v[236:239], v[120:127], v1\n"
"ds_read_b128 v[248:251], v2 offset:26624\n"
"v_exp_f32 v206, v206\n"
"v_smfmac_f32_16x16x64_bf16 v[196:199], v[236:239], v[152:159], v1\n"
"ds_read_b128 v[252:255], v2 offset:27648\n"
"v_exp_f32 v207, v207\n"
"v_cvt_pk_bf16_f32 v40, v200, v201\n"
"v_cvt_pk_bf16_f32 v41, v202, v203\n"
"v_cvt_pk_bf16_f32 v42, v204, v205\n"
"v_cvt_pk_bf16_f32 v43, v206, v207\n"
"v_pk_fma_f32 v[216:217], v[216:217], s[58:59], v[92:93] op_sel_hi:[1,0,0] neg_lo:[0,0,1] neg_hi:[0,0,1]\n"
"v_pk_fma_f32 v[218:219], v[218:219], s[58:59], v[92:93] op_sel_hi:[1,0,0] neg_lo:[0,0,1] neg_hi:[0,0,1]\n"
"v_mfma_f32_16x16x32_bf16 v[220:223], v[40:43], v[88:91], 0\n"
"v_exp_f32 v216, v216\n"
"s_waitcnt lgkmcnt(2)\n"
"v_smfmac_f32_16x16x64_bf16 v[208:211], v[40:43], v[240:247], v1\n"
"ds_read_b128 v[224:227], v9 offset:8192\n"
"v_exp_f32 v217, v217\n"
"s_waitcnt lgkmcnt(1)\n"
"v_smfmac_f32_16x16x64_bf16 v[212:215], v[40:43], v[248:255], v1\n"
"ds_read_b128 v[228:231], v9 offset:9216\n"
"v_exp_f32 v218, v218\n"
"v_exp_f32 v219, v219\n"
"v_cvt_pk_bf16_f32 v200, v192, v193\n"
"v_cvt_pk_bf16_f32 v44, v216, v217\n"
"v_cvt_pk_bf16_f32 v45, v218, v219\n"
"v_cndmask_b32 v44, 0, v44, s[50:51]\n"
"v_cndmask_b32 v45, 0, v45, s[50:51]\n"
"v_cvt_pk_bf16_f32 v201, v194, v195\n"
"s_nop 0\n"
"v_mfma_f32_16x16x16_bf16 v[220:223], v[44:45], v[88:89], v[220:223]\n"
"v_cvt_pk_bf16_f32 v202, v196, v197\n"
"v_cvt_pk_bf16_f32 v203, v198, v199\n"
"s_nop 0\n"
"v_mfma_f32_16x16x16_bf16 v[208:211], v[44:45], v[200:201], v[208:211]\n"
"s_nop 0\n"
"s_cmp_eq_u32 %11, 3\n"
"s_nop 0\n"
"v_mfma_f32_16x16x16_bf16 v[212:215], v[44:45], v[202:203], v[212:215]\n"
"v_rcp_f32 v220, v220\n"
"v_rcp_f32 v221, v221\n"
"v_rcp_f32 v222, v222\n"
"v_rcp_f32 v223, v223\n"
"v_pk_mul_f32 v[208:209], v[208:209], v[220:221]\n"
"v_pk_mul_f32 v[210:211], v[210:211], v[222:223]\n"
"s_nop 1\n"
"v_pk_mul_f32 v[212:213], v[212:213], v[220:221]\n"
"v_pk_mul_f32 v[214:215], v[214:215], v[222:223]\n"
"v_cvt_pk_bf16_f32 v80, v208, v212\n"
"v_cvt_pk_bf16_f32 v81, v209, v213\n"
"v_cvt_pk_bf16_f32 v82, v210, v214\n"
"v_cvt_pk_bf16_f32 v83, v211, v215\n"
"v_min_u32 v80, -2, v80\n"
"v_min_u32 v81, -2, v81\n"
"v_min_u32 v82, -2, v82\n"
"v_min_u32 v83, -2, v83\n"
"buffer_store_dwordx4 v[80:83], v11, %7, s44 offen sc1\n"
"s_cbranch_scc1 CH_PY%=\n"
"s_memrealtime s[90:91]\n"
"ds_read_b128 v[240:243], v9 offset:12288\n"
"ds_read_b128 v[244:247], v9 offset:13312\n"
"ds_read_b128 v[232:235], v9 offset:10240\n"
"ds_read_b128 v[248:251], v9 offset:14336\n"
"ds_read_b128 v[236:239], v9 offset:11264\n"
"ds_read_b128 v[252:255], v9 offset:15360\n"
"s_add_u32 s54, %11, s94\n"
"s_cmp_lg_u32 s54, 0\n"
"s_cbranch_scc1 CH_RA%=\n"
"buffer_load_dword v14, v13, %7, 0 offen sc1\n"
"CH_RA%=:\n"
"CH_END%=:\n"
"s_cmp_lg_u32 %11, 2\n"
"s_cbranch_scc1 CH_RB%=\n"
"ds_read_b128 v[72:75], v14 offset:0\n"
"ds_read_b128 v[76:79], v14 offset:1152\n"
"s_waitcnt lgkmcnt(1)\n"
"v_min_u32 v72, -2, v72\n"
"v_min_u32 v73, -2, v73\n"
"v_min_u32 v74, -2, v74\n"
"v_min_u32 v75, -2, v75\n"
"s_waitcnt lgkmcnt(0)\n"
"v_min_u32 v76, -2, v76\n"
"v_min_u32 v77, -2, v77\n"
"v_min_u32 v78, -2, v78\n"
"v_min_u32 v79, -2, v79\n"
"buffer_store_dwordx4 v[72:75], v13, %7, s47 offen sc1\n"
"buffer_store_dwordx4 v[76:79], v13, %7, s47 offen offset:1024 sc1\n"
"CH_RB%=:\n"
"s_cmp_eq_u32 s40, 63\n"
"s_cbranch_scc1 CH_FIN%=\n"
"v_perm_b32 v48, v97, v96, s56\n"
"v_perm_b32 v49, v99, v98, s56\n"
"v_perm_b32 v50, v101, v100, s56\n"
"v_perm_b32 v51, v103, v102, s56\n"
"v_perm_b32 v52, v97, v96, s57\n"
"v_perm_b32 v53, v99, v98, s57\n"
"v_perm_b32 v54, v101, v100, s57\n"
"v_perm_b32 v55, v103, v102, s57\n"
"s_waitcnt lgkmcnt(7)\n"
"v_mfma_f32_16x16x32_bf16 v[192:195], v[224:227], v[48:51], 0\n"
"ds_read_b128 v[16:19], v9 offset:0\n"
"v_perm_b32 v56, v105, v104, s56\n"
"v_perm_b32 v57, v107, v106, s56\n"
"s_waitcnt lgkmcnt(6)\n"
"v_mfma_f32_16x16x32_bf16 v[200:203], v[240:243], v[48:51], 0\n"
"ds_read_b128 v[20:23], v9 offset:1024\n"
"v_perm_b32 v58, v109, v108, s56\n"
"v_perm_b32 v59, v111, v110, s56\n"
"v_mfma_f32_16x16x32_bf16 v[196:199], v[224:227], v[52:55], 0\n"
"ds_read_b128 v[24:27], v9 offset:2048\n"
"v_perm_b32 v60, v105, v104, s57\n"
"v_perm_b32 v61, v107, v106, s57\n"
"v_mfma_f32_16x16x32_bf16 v[204:207], v[240:243], v[52:55], 0\n"
"ds_read_b128 v[28:31], v9 offset:3072\n"
"v_perm_b32 v62, v109, v108, s57\n"
"v_perm_b32 v63, v111, v110, s57\n"
"v_mfma_f32_16x16x32_bf16 v[192:195], v[228:231], v[56:59], v[192:195]\n"
"ds_read_b128 v[32:35], v9 offset:4096\n"
"v_perm_b32 v48, v113, v112, s56\n"
"v_perm_b32 v49, v115, v114, s56\n"
"s_waitcnt lgkmcnt(9)\n"
"v_mfma_f32_16x16x32_bf16 v[200:203], v[244:247], v[56:59], v[200:203]\n"
"ds_read_b128 v[36:39], v9 offset:5120\n"
"v_perm_b32 v50, v117, v116, s56\n"
"v_perm_b32 v51, v119, v118, s56\n"
"v_mfma_f32_16x16x32_bf16 v[196:199], v[228:231], v[60:63], v[196:199]\n"
"v_perm_b32 v52, v113, v112, s57\n"
"v_perm_b32 v53, v115, v114, s57\n"
"v_mfma_f32_16x16x32_bf16 v[204:207], v[244:247], v[60:63], v[204:207]\n"
"v_perm_b32 v54, v117, v116, s57\n"
"v_perm_b32 v55, v119, v118, s57\n"
"s_waitcnt lgkmcnt(9)\n"
"v_mfma_f32_16x16x32_bf16 v[192:195], v[232:235], v[48:51], v[192:195]\n"
"v_perm_b32 v56, v121, v120, s56\n"
"v_perm_b32 v57, v123, v122, s56\n"
"s_waitcnt lgkmcnt(8)\n"
"v_mfma_f32_16x16x32_bf16 v[200:203], v[248:251], v[48:51], v[200:203]\n"
"v_perm_b32 v58, v125, v124, s56\n"
"v_perm_b32 v59, v127, v126, s56\n"
"v_mfma_f32_16x16x32_bf16 v[196:199], v[232:235], v[52:55], v[196:199]\n"
"v_perm_b32 v60, v121, v120, s57\n"
"v_perm_b32 v61, v123, v122, s57\n"
"v_mfma_f32_16x16x32_bf16 v[204:207], v[248:251], v[52:55], v[204:207]\n"
"v_perm_b32 v62, v125, v124, s57\n"
"v_perm_b32 v63, v127, v126, s57\n"
"s_waitcnt lgkmcnt(7)\n"
"v_mfma_f32_16x16x32_bf16 v[192:195], v[236:239], v[56:59], v[192:195]\n"
"v_perm_b32 v48, v129, v128, s56\n"
"v_perm_b32 v49, v131, v130, s56\n"
"v_mfma_f32_16x16x32_bf16 v[196:199], v[236:239], v[60:63], v[196:199]\n"
"v_perm_b32 v50, v133, v132, s56\n"
"v_perm_b32 v51, v135, v134, s56\n"
"s_waitcnt lgkmcnt(6)\n"
"v_mfma_f32_16x16x32_bf16 v[200:203], v[252:255], v[56:59], v[200:203]\n"
"v_perm_b32 v52, v129, v128, s57\n"
"v_perm_b32 v53, v131, v130, s57\n"
"v_mfma_f32_16x16x32_bf16 v[204:207], v[252:255], v[60:63], v[204:207]\n"
"v_perm_b32 v54, v133, v132, s57\n"
"v_perm_b32 v55, v135, v134, s57\n"
"v_cvt_pk_bf16_f32 v40, v192, v196\n"
"v_cvt_pk_bf16_f32 v41, v193, v197\n"
"v_cvt_pk_bf16_f32 v42, v194, v198\n"
"v_cvt_pk_bf16_f32 v43, v195, v199\n"
"s_nop 1\n"
"v_cvt_pk_bf16_f32 v44, v200, v204\n"
"v_cvt_pk_bf16_f32 v45, v201, v205\n"
"v_cvt_pk_bf16_f32 v46, v202, v206\n"
"v_cvt_pk_bf16_f32 v47, v203, v207\n"
"v_mfma_f32_16x16x32_bf16 v[192:195], v[224:227], v[48:51], 0\n"
"ds_write_b128 v2, v[40:43] offset:24576\n"
"v_perm_b32 v56, v137, v136, s56\n"
"v_perm_b32 v57, v139, v138, s56\n"
"v_mfma_f32_16x16x32_bf16 v[200:203], v[240:243], v[48:51], 0\n"
"ds_write_b128 v2, v[44:47] offset:25600\n"
"v_perm_b32 v58, v141, v140, s56\n"
"v_perm_b32 v59, v143, v142, s56\n"
"v_mfma_f32_16x16x32_bf16 v[196:199], v[224:227], v[52:55], 0\n"
"v_perm_b32 v60, v137, v136, s57\n"
"v_perm_b32 v61, v139, v138, s57\n"
"v_mfma_f32_16x16x32_bf16 v[204:207], v[240:243], v[52:55], 0\n"
"v_perm_b32 v62, v141, v140, s57\n"
"v_perm_b32 v63, v143, v142, s57\n"
"v_mfma_f32_16x16x32_bf16 v[192:195], v[228:231], v[56:59], v[192:195]\n"
"ds_read_b128 v[224:227], v2 offset:0\n"
"v_perm_b32 v48, v145, v144, s56\n"
"v_perm_b32 v49, v147, v146, s56\n"
"v_mfma_f32_16x16x32_bf16 v[200:203], v[244:247], v[56:59], v[200:203]\n"
"ds_read_b128 v[240:243], v2 offset:4096\n"
"v_perm_b32 v50, v149, v148, s56\n"
"v_perm_b32 v51, v151, v150, s56\n"
"v_mfma_f32_16x16x32_bf16 v[196:199], v[228:231], v[60:63], v[196:199]\n"
"v_perm_b32 v52, v145, v144, s57\n"
"v_perm_b32 v53, v147, v146, s57\n"
"v_mfma_f32_16x16x32_bf16 v[204:207], v[244:247], v[60:63], v[204:207]\n"
"v_perm_b32 v54, v149, v148, s57\n"
"v_perm_b32 v55, v151, v150, s57\n"
"v_mfma_f32_16x16x32_bf16 v[192:195], v[232:235], v[48:51], v[192:195]\n"
"ds_read_b128 v[228:231], v2 offset:1024\n"
"v_perm_b32 v56, v153, v152, s56\n"
"v_perm_b32 v57, v155, v154, s56\n"
"v_mfma_f32_16x16x32_bf16 v[200:203], v[248:251], v[48:51], v[200:203]\n"
"ds_read_b128 v[244:247], v2 offset:5120\n"
"v_perm_b32 v58, v157, v156, s56\n"
"v_perm_b32 v59, v159, v158, s56\n"
"s_add_u32 s54, %11, s94\n"
"s_cmp_lg_u32 s54, 0\n"
"s_cbranch_scc1 CH_FK2%=\n"
"s_add_u32 s54, s40, 4\n"
"s_lshr_b32 s54, s54, 1\n"
"s_min_u32 s54, s54, 32\n"
"CH_FK%=:\n"
"s_waitcnt vmcnt(0)\n"
"v_subrev_u32 v14, s38, v14\n"
"v_cmp_gt_u32 vcc, 32, v14\n"
"s_cmp_eq_u64 vcc, 0\n"
"s_cselect_b32 s94, 8, 0\n"
"v_cmp_gt_u32 vcc, s54, v14\n"
"s_cbranch_vccz CH_FK2%=\n"
"s_sleep 2\n"
"buffer_load_dword v14, v13, %7, 0 offen sc1\n"
"s_add_u32 s49, s49, 1\n"
"s_cmp_gt_u32 s49, 0x10000\n"
"s_cbranch_scc0 CH_FK%=\n"
"CH_FK2%=:\n"
"s_barrier\n"
"v_mfma_f32_16x16x32_bf16 v[196:199], v[232:235], v[52:55], v[196:199]\n"
"v_perm_b32 v60, v153, v152, s57\n"
"v_perm_b32 v61, v155, v154, s57\n"
"v_mfma_f32_16x16x32_bf16 v[204:207], v[248:251], v[52:55], v[204:207]\n"
"v_perm_b32 v62, v157, v156, s57\n"
"v_perm_b32 v63, v159, v158, s57\n"
"v_mfma_f32_16x16x32_bf16 v[192:195], v[236:239], v[56:59], v[192:195]\n"
"ds_read_b128 v[232:235], v2 offset:2048\n"
"v_pk_mov_b32 v[48:49], 0, 0\n"
"v_mfma_f32_16x16x32_bf16 v[196:199], v[236:239], v[60:63], v[196:199]\n"
"ds_read_b128 v[248:251], v2 offset:6144\n"
"v_pk_mov_b32 v[50:51], 0, 0\n"
"v_mfma_f32_16x16x32_bf16 v[200:203], v[252:255], v[56:59], v[200:203]\n"
"v_pk_mov_b32 v[52:53], 0, 0\n"
"v_mfma_f32_16x16x32_bf16 v[204:207], v[252:255], v[60:63], v[204:207]\n"
"v_pk_mov_b32 v[54:55], 0, 0\n"
"ds_read_b128 v[236:239], v2 offset:3072\n"
"ds_read_b128 v[252:255], v2 offset:7168\n"
"v_pk_mov_b32 v[56:57], 0, 0\n"
"v_pk_mov_b32 v[58:59], 0, 0\n"
"v_pk_mov_b32 v[60:61], 0, 0\n"
"v_pk_mov_b32 v[62:63], 0, 0\n"
"v_cvt_pk_bf16_f32 v40, v192, v196\n"
"v_cvt_pk_bf16_f32 v41, v193, v197\n"
"v_cvt_pk_bf16_f32 v42, v194, v198\n"
"v_cvt_pk_bf16_f32 v43, v195, v199\n"
"v_cvt_pk_bf16_f32 v44, v200, v204\n"
"v_cvt_pk_bf16_f32 v45, v201, v205\n"
"v_cvt_pk_bf16_f32 v46, v202, v206\n"
"v_cvt_pk_bf16_f32 v47, v203, v207\n"
"ds_write_b128 v2, v[40:43] offset:26624\n"
"ds_write_b128 v2, v[44:47] offset:27648\n"
"ds_read_b128 v[192:195], v2 offset:8192\n"
"ds_read_b128 v[196:199], v2 offset:9216\n"
"ds_read_b128 v[200:203], v2 offset:10240\n"
"ds_read_b128 v[204:207], v2 offset:11264\n"
"v_pk_mov_b32 v[40:41], 0, 0\n"
"v_pk_mov_b32 v[42:43], 0, 0\n"
"v_pk_mov_b32 v[44:45], 0, 0\n"
"v_pk_mov_b32 v[46:47], 0, 0\n"
"s_sleep 1\n"
"s_nop 8\n"
"buffer_load_dwordx4 v[64:67], v12, %7, s53 offen sc1\n"
"buffer_load_dwordx4 v[68:71], v12, %7, s53 offen offset:1024 sc1\n"
"s_add_u32 s95, s95, 1\n"
"s_add_u32 s47, s47, 4096\n"
"s_add_u32 s78, s78, 32768\n"
"s_add_u32 s40, s40, 1\n"
"s_xor_b32 s44, s44, 0x80000\n"
"s_sub_u32 s48, s59, s48\n"
"s_add_u32 s46, s46, 32768\n"
"s_mov_b32 s43, s53\n"
"s_add_u32 s53, s53, 0x10000\n"
"s_and_b32 s53, s53, 0x3ffff\n"
"s_xor_b32 s45, s53, 0x20000\n"
"s_add_u32 s52, s53, 0x8000\n"
"s_add_u32 s77, s45, s37\n"
"s_sub_u32 s93, s79, s93\n"
"s_waitcnt lgkmcnt(0)\n"
"s_sub_u32 s54, s90, s60\n"
"s_lshl_b32 s54, s54, 4\n"
"s_sub_i32 s54, s54, s92\n"
"s_min_i32 s54, s54, 128\n"
"s_max_i32 s54, s54, -128\n"
"s_ashr_i32 s54, s54, 2\n"
"s_add_i32 s92, s92, s54\n"
"s_lshr_b32 s76, s92, 4\n"
"s_branch CH_LOOP%=\n"
"CH_PY%=:\n"
"ds_read_b128 v[240:243], v9 offset:12288\n"
"ds_read_b128 v[244:247], v9 offset:13312\n"
"s_waitcnt vmcnt(1)\n"
"v_readlane_b32 s60, v95, s41\n"
"s_getpc_b64 s[88:89]\n"
"CH_PC%=:\n"
"s_add_u32 s88, s88, CH_SE%=-CH_PC%=\n"
"v_subrev_u32 v95, s60, v95\n"
"v_cmp_gt_u32 vcc, 40, v95\n"
"s_nop 0\n"
"s_nop 0\n"
"v_cndmask_b32 v95, 0, v95, vcc\n"
"s_addc_u32 s89, s89, 0\n"
"s_nop 0\n"
"v_max_u32_dpp v95, v95, v95 row_shr:1 row_mask:0xf bank_mask:0xf\n"
"v_perm_b32 v48, v97, v96, s56\n"
"v_perm_b32 v49, v99, v98, s56\n"
"v_max_u32_dpp v95, v95, v95 row_shr:2 row_mask:0xf bank_mask:0xf\n"
"v_perm_b32 v50, v101, v100, s56\n"
"v_perm_b32 v51, v103, v102, s56\n"
"v_max_u32_dpp v95, v95, v95 row_shr:4 row_mask:0xf bank_mask:0xf\n"
"v_perm_b32 v52, v97, v96, s57\n"
"v_perm_b32 v53, v99, v98, s57\n"
"v_max_u32_dpp v95, v95, v95 row_shr:8 row_mask:0xf bank_mask:0xf\n"
"v_perm_b32 v54, v101, v100, s57\n"
"v_perm_b32 v55, v103, v102, s57\n"
"v_max_u32_dpp v95, v95, v95 row_bcast:15 row_mask:0xa bank_mask:0xf\n"
"s_nop 1\n"
"v_max_u32_dpp v95, v95, v95 row_bcast:31 row_mask:0xc bank_mask:0xf\n"
"s_waitcnt lgkmcnt(3)\n"
"v_mfma_f32_16x16x32_bf16 v[192:195], v[224:227], v[48:51], 0\n"
"ds_read_b128 v[232:235], v9 offset:10240\n"
"v_perm_b32 v56, v105, v104, s56\n"
"v_perm_b32 v57, v107, v106, s56\n"
"s_nop 0\n"
"v_readlane_b32 s54, v95, 63\n"
"s_waitcnt lgkmcnt(2)\n"
"v_mfma_f32_16x16x32_bf16 v[200:203], v[240:243], v[48:51], 0\n"
"ds_read_b128 v[248:251], v9 offset:14336\n"
"v_perm_b32 v58, v109, v108, s56\n"
"v_perm_b32 v59, v111, v110, s56\n"
"s_lshl_b32 s54, s54, 2\n"
"s_sub_u32 s88, s88, s54\n"
"s_subb_u32 s89, s89, 0\n"
"s_setpc_b64 s[88:89]\n"
"s_nop  5\n"
"s_nop  5\n"
"s_nop  5\n"
"s_nop  5\n"
"s_nop  5\n"
"s_nop  5\n"
"s_nop  5\n"
"s_nop  5\n"
"s_nop  5\n"
"s_nop  5\n"
"s_nop  5\n"
"s_nop  5\n"
"s_nop  5\n"
"s_nop  5\n"
"s_nop  5\n"
"s_nop  5\n"
"s_nop  5\n"
"s_nop  5\n"
"s_nop  5\n"
"s_nop  5\n"
"s_nop  5\n"
"s_nop  5\n"
"s_nop  5\n"
"s_nop  5\n"
"s_nop  5\n"
"s_nop  5\n"
"s_nop  5\n"
"s_nop  5\n"
"s_nop  5\n"
"s_nop  5\n"
"s_nop  5\n"
"s_nop  5\n"
"s_nop  5\n"
"s_nop  5\n"
"s_nop  5\n"
"s_nop  5\n"
"s_nop  5\n"
"s_nop  5\n"
"s_nop  5\n"
"s_nop  5\n"
"CH_SE%=:\n"
"buffer_load_dwordx4 v[208:211], v13, %7, s44 offen offset:0 sc1\n"
"buffer_load_dwordx4 v[212:215], v13, %7, s44 offen offset:1024 sc1\n"
"buffer_load_dwordx4 v[216:219], v13, %7, s44 offen offset:2048 sc1\n"
"buffer_load_dwordx4 v[220:223], v13, %7, s44 offen offset:3072 sc1\n"
"v_cndmask_b32 v88, 0, v10, s[80:81]\n"
"v_cndmask_b32 v89, 0, v10, s[82:83]\n"
"v_cndmask_b32 v90, 0, v10, s[84:85]\n"
"v_cndmask_b32 v91, 0, v10, s[86:87]\n"
"v_pk_mov_b32 v[76:77], -1, -1\n"
"v_pk_mov_b32 v[78:79], -1, -1\n"
"v_mfma_f32_16x16x32_bf16 v[196:199], v[224:227], v[52:55], 0\n"
"ds_read_b128 v[236:239], v9 offset:11264\n"
"v_perm_b32 v60, v105, v104, s57\n"
"v_perm_b32 v61, v107, v106, s57\n"
"v_mfma_f32_16x16x32_bf16 v[204:207], v[240:243], v[52:55], 0\n"
"ds_read_b128 v[252:255], v9 offset:15360\n"
"v_perm_b32 v62, v109, v108, s57\n"
"v_perm_b32 v63, v111, v110, s57\n"
"v_mfma_f32_16x16x32_bf16 v[192:195], v[228:231], v[56:59], v[192:195]\n"
"ds_read_b128 v[16:19], v9 offset:0\n"
"v_perm_b32 v48, v113, v112, s56\n"
"v_perm_b32 v49, v115, v114, s56\n"
"s_waitcnt lgkmcnt(5)\n"
"v_mfma_f32_16x16x32_bf16 v[200:203], v[244:247], v[56:59], v[200:203]\n"
"ds_read_b128 v[20:23], v9 offset:1024\n"
"v_perm_b32 v50, v117, v116, s56\n"
"v_perm_b32 v51, v119, v118, s56\n"
"v_mfma_f32_16x16x32_bf16 v[196:199], v[228:231], v[60:63], v[196:199]\n"
"ds_read_b128 v[24:27], v9 offset:2048\n"
"v_perm_b32 v52, v113, v112, s57\n"
"v_perm_b32 v53, v115, v114, s57\n"
"v_mfma_f32_16x16x32_bf16 v[204:207], v[244:247], v[60:63], v[204:207]\n"
"ds_read_b128 v[28:31], v9 offset:3072\n"
"v_perm_b32 v54, v117, v116, s57\n"
"v_perm_b32 v55, v119, v118, s57\n"
"s_waitcnt lgkmcnt(7)\n"
"v_mfma_f32_16x16x32_bf16 v[192:195], v[232:235], v[48:51], v[192:195]\n"
"ds_read_b128 v[32:35], v9 offset:4096\n"
"v_perm_b32 v56, v121, v120, s56\n"
"v_perm_b32 v57, v123, v122, s56\n"
"s_waitcnt lgkmcnt(7)\n"
"v_mfma_f32_16x16x32_bf16 v[200:203], v[248:251], v[48:51], v[200:203]\n"
"ds_read_b128 v[36:39], v9 offset:5120\n"
"v_perm_b32 v58, v125, v124, s56\n"
"v_perm_b32 v59, v127, v126, s56\n"
"v_mfma_f32_16x16x32_bf16 v[196:199], v[232:235], v[52:55], v[196:199]\n"
"v_perm_b32 v60, v121, v120, s57\n"
"v_perm_b32 v61, v123, v122, s57\n"
"v_mfma_f32_16x16x32_bf16 v[204:207], v[248:251], v[52:55], v[204:207]\n"
"v_perm_b32 v62, v125, v124, s57\n"
"v_perm_b32 v63, v127, v126, s57\n"
"s_waitcnt lgkmcnt(7)\n"
"v_mfma_f32_16x16x32_bf16 v[192:195], v[236:239], v[56:59], v[192:195]\n"
"v_perm_b32 v48, v129, v128, s56\n"
"v_perm_b32 v49, v131, v130, s56\n"
"v_mfma_f32_16x16x32_bf16 v[196:199], v[236:239], v[60:63], v[196:199]\n"
"v_perm_b32 v50, v133, v132, s56\n"
"v_perm_b32 v51, v135, v134, s56\n"
"s_waitcnt lgkmcnt(6)\n"
"v_mfma_f32_16x16x32_bf16 v[200:203], v[252:255], v[56:59], v[200:203]\n"
"v_perm_b32 v52, v129, v128, s57\n"
"v_perm_b32 v53, v131, v130, s57\n"
"v_mfma_f32_16x16x32_bf16 v[204:207], v[252:255], v[60:63], v[204:207]\n"
"v_perm_b32 v54, v133, v132, s57\n"
"v_perm_b32 v55, v135, v134, s57\n"
"v_cvt_pk_bf16_f32 v40, v192, v196\n"
"v_cvt_pk_bf16_f32 v41, v193, v197\n"
"v_cvt_pk_bf16_f32 v42, v194, v198\n"
"v_cvt_pk_bf16_f32 v43, v195, v199\n"
"s_nop 1\n"
"v_cvt_pk_bf16_f32 v44, v200, v204\n"
"v_cvt_pk_bf16_f32 v45, v201, v205\n"
"v_cvt_pk_bf16_f32 v46, v202, v206\n"
"v_cvt_pk_bf16_f32 v47, v203, v207\n"
"v_mfma_f32_16x16x32_bf16 v[192:195], v[224:227], v[48:51], 0\n"
"ds_write_b128 v2, v[40:43] offset:24576\n"
"v_perm_b32 v56, v137, v136, s56\n"
"v_perm_b32 v57, v139, v138, s56\n"
"v_mfma_f32_16x16x32_bf16 v[200:203], v[240:243], v[48:51], 0\n"
"ds_write_b128 v2, v[44:47] offset:25600\n"
"v_perm_b32 v58, v141, v140, s56\n"
"v_perm_b32 v59, v143, v142, s56\n"
"s_branch CH_R3%=\n"
"CH_YRE%=:\n"
"buffer_load_dwordx4 v[208:211], v13, %7, s44 offen offset:0 sc1\n"
"buffer_load_dwordx4 v[212:215], v13, %7, s44 offen offset:1024 sc1\n"
"buffer_load_dwordx4 v[216:219], v13, %7, s44 offen offset:2048 sc1\n"
"buffer_load_dwordx4 v[220:223], v13, %7, s44 offen offset:3072 sc1\n"
"s_add_u32 s49, s49, 1\n"
"s_cmp_gt_u32 s49, 0x10000\n"
"s_cbranch_scc1 CH_YOK%=\n"
"CH_R3%=:\n"
"s_waitcnt vmcnt(3)\n"
"v_mfma_f32_16x16x32_bf16 v[80:83], v[88:91], v[208:211], 0\n"
"v_max3_u32 v84, v208, v209, v210\n"
"v_max_u32 v84, v84, v211\n"
"s_waitcnt vmcnt(2)\n"
"v_mfma_f32_16x16x32_bf16 v[80:83], v[88:91], v[212:215], v[80:83]\n"
"v_max3_u32 v84, v84, v212, v213\n"
"v_max3_u32 v84, v84, v214, v215\n"
"s_waitcnt vmcnt(1)\n"
"v_mfma_f32_16x16x32_bf16 v[80:83], v[88:91], v[216:219], v[80:83]\n"
"v_max3_u32 v84, v84, v216, v217\n"
"v_max3_u32 v84, v84, v218, v219\n"
"s_waitcnt vmcnt(0)\n"
"v_mfma_f32_16x16x32_bf16 v[80:83], v[88:91], v[220:223], v[80:83]\n"
"v_max3_u32 v84, v84, v220, v221\n"
"v_max3_u32 v84, v84, v222, v223\n"
"v_cmp_eq_u32 vcc, -1, v84\n"
"s_nop 3\n"
"s_cbranch_vccnz CH_YRE%=\n"
"CH_YOK%=:\n"
"v_add_f32_dpp v82, v80, v80 row_ror:8 row_mask:0xf bank_mask:0xf\n"
"v_add_f32_dpp v83, v81, v81 row_ror:8 row_mask:0xf bank_mask:0xf\n"
"v_cvt_pk_bf16_f32 v80, v82, v83\n"
"v_min_u32 v80, -2, v80\n"
"buffer_store_dword v80, v14, %7, s53 offen sc1\n"
"buffer_store_dword v80, v14, %7, s52 offen sc1\n"
"s_barrier\n"
"s_xor_b32 s52, s52, 0x20000\n"
"buffer_store_dword v76, v14, %7, s45 offen sc1\n"
"buffer_store_dword v76, v14, %7, s52 offen sc1\n"
"buffer_store_dwordx4 v[76:79], v13, %7, s44 offen offset:0 sc1\n"
"buffer_store_dwordx4 v[76:79], v13, %7, s44 offen offset:1024 sc1\n"
"buffer_store_dwordx4 v[76:79], v13, %7, s44 offen offset:2048 sc1\n"
"buffer_store_dwordx4 v[76:79], v13, %7, s44 offen offset:3072 sc1\n"
"v_mfma_f32_16x16x32_bf16 v[196:199], v[224:227], v[52:55], 0\n"
"v_perm_b32 v60, v137, v136, s57\n"
"v_perm_b32 v61, v139, v138, s57\n"
"v_mfma_f32_16x16x32_bf16 v[204:207], v[240:243], v[52:55], 0\n"
"v_perm_b32 v62, v141, v140, s57\n"
"v_perm_b32 v63, v143, v142, s57\n"
"v_mfma_f32_16x16x32_bf16 v[192:195], v[228:231], v[56:59], v[192:195]\n"
"ds_read_b128 v[224:227], v2 offset:0\n"
"v_perm_b32 v48, v145, v144, s56\n"
"v_perm_b32 v49, v147, v146, s56\n"
"v_mfma_f32_16x16x32_bf16 v[200:203], v[244:247], v[56:59], v[200:203]\n"
"ds_read_b128 v[240:243], v2 offset:4096\n"
"v_perm_b32 v50, v149, v148, s56\n"
"v_perm_b32 v51, v151, v150, s56\n"
"v_mfma_f32_16x16x32_bf16 v[196:199], v[228:231], v[60:63], v[196:199]\n"
"v_perm_b32 v52, v145, v144, s57\n"
"v_perm_b32 v53, v147, v146, s57\n"
"v_mfma_f32_16x16x32_bf16 v[204:207], v[244:247], v[60:63], v[204:207]\n"
"v_perm_b32 v54, v149, v148, s57\n"
"v_perm_b32 v55, v151, v150, s57\n"
"v_mfma_f32_16x16x32_bf16 v[192:195], v[232:235], v[48:51], v[192:195]\n"
"ds_read_b128 v[228:231], v2 offset:1024\n"
"v_perm_b32 v56, v153, v152, s56\n"
"v_perm_b32 v57, v155, v154, s56\n"
"v_mfma_f32_16x16x32_bf16 v[200:203], v[248:251], v[48:51], v[200:203]\n"
"ds_read_b128 v[244:247], v2 offset:5120\n"
"v_perm_b32 v58, v157, v156, s56\n"
"v_perm_b32 v59, v159, v158, s56\n"
"v_mfma_f32_16x16x32_bf16 v[196:199], v[232:235], v[52:55], v[196:199]\n"
"v_perm_b32 v60, v153, v152, s57\n"
"v_perm_b32 v61, v155, v154, s57\n"
"v_mfma_f32_16x16x32_bf16 v[204:207], v[248:251], v[52:55], v[204:207]\n"
"v_perm_b32 v62, v157, v156, s57\n"
"v_perm_b32 v63, v159, v158, s57\n"
"v_mfma_f32_16x16x32_bf16 v[192:195], v[236:239], v[56:59], v[192:195]\n"
"ds_read_b128 v[232:235], v2 offset:2048\n"
"v_pk_mov_b32 v[48:49], 0, 0\n"
"v_mfma_f32_16x16x32_bf16 v[196:199], v[236:239], v[60:63], v[196:199]\n"
"ds_read_b128 v[248:251], v2 offset:6144\n"
"v_pk_mov_b32 v[50:51], 0, 0\n"
"buffer_load_dwordx4 v[64:67], v12, %7, s53 offen sc1\n"
"buffer_load_dwordx4 v[68:71], v12, %7, s53 offen offset:1024 sc1\n"
"s_add_u32 s95, s95, 1\n"
"s_add_u32 s47, s47, 4096\n"
"s_add_u32 s78, s78, 32768\n"
"s_add_u32 s40, s40, 1\n"
"s_xor_b32 s44, s44, 0x80000\n"
"s_sub_u32 s48, s59, s48\n"
"s_add_u32 s46, s46, 32768\n"
"s_mov_b32 s43, s53\n"
"s_add_u32 s53, s53, 0x10000\n"
"s_and_b32 s53, s53, 0x3ffff\n"
"s_xor_b32 s45, s53, 0x20000\n"
"s_add_u32 s52, s53, 0x8000\n"
"s_add_u32 s77, s45, s37\n"
"s_sub_u32 s93, s79, s93\n"
"v_mfma_f32_16x16x32_bf16 v[200:203], v[252:255], v[56:59], v[200:203]\n"
"v_pk_mov_b32 v[52:53], 0, 0\n"
"v_mfma_f32_16x16x32_bf16 v[204:207], v[252:255], v[60:63], v[204:207]\n"
"v_pk_mov_b32 v[54:55], 0, 0\n"
"ds_read_b128 v[236:239], v2 offset:3072\n"
"ds_read_b128 v[252:255], v2 offset:7168\n"
"v_pk_mov_b32 v[56:57], 0, 0\n"
"v_pk_mov_b32 v[58:59], 0, 0\n"
"v_pk_mov_b32 v[60:61], 0, 0\n"
"v_pk_mov_b32 v[62:63], 0, 0\n"
"v_cvt_pk_bf16_f32 v40, v192, v196\n"
"v_cvt_pk_bf16_f32 v41, v193, v197\n"
"v_cvt_pk_bf16_f32 v42, v194, v198\n"
"v_cvt_pk_bf16_f32 v43, v195, v199\n"
"v_cvt_pk_bf16_f32 v44, v200, v204\n"
"v_cvt_pk_bf16_f32 v45, v201, v205\n"
"v_cvt_pk_bf16_f32 v46, v202, v206\n"
"v_cvt_pk_bf16_f32 v47, v203, v207\n"
"ds_write_b128 v2, v[40:43] offset:26624\n"
"ds_write_b128 v2, v[44:47] offset:27648\n"
"ds_read_b128 v[192:195], v2 offset:8192\n"
"ds_read_b128 v[196:199], v2 offset:9216\n"
"ds_read_b128 v[200:203], v2 offset:10240\n"
"ds_read_b128 v[204:207], v2 offset:11264\n"
"v_pk_mov_b32 v[40:41], 0, 0\n"
"v_pk_mov_b32 v[42:43], 0, 0\n"
"v_pk_mov_b32 v[44:45], 0, 0\n"
"v_pk_mov_b32 v[46:47], 0, 0\n"
"s_branch CH_LOOP%=\n"
"CH_RETRY%=:\n"
"buffer_load_dwordx4 v[64:67], v12, %7, s43 offen sc1\n"
"buffer_load_dwordx4 v[68:71], v12, %7, s43 offen offset:1024 sc1\n"
"s_nop 7\n"
"v_pk_mov_b32 v[48:49], 0, 0\n"
"v_pk_mov_b32 v[50:51], 0, 0\n"
"v_pk_mov_b32 v[52:53], 0, 0\n"
"v_pk_mov_b32 v[54:55], 0, 0\n"
"v_pk_mov_b32 v[56:57], 0, 0\n"
"v_pk_mov_b32 v[58:59], 0, 0\n"
"v_pk_mov_b32 v[60:61], 0, 0\n"
"v_pk_mov_b32 v[62:63], 0, 0\n"
"s_add_u32 s49, s49, 1\n"
"s_cmp_gt_u32 s49, 0x10000\n"
"s_waitcnt vmcnt(0)\n"
"s_cbranch_scc0 CH_LOOP%=\n"
"v_mov_b32 v64, 0x7fc07fc0\n"
"v_mov_b32 v65, 0x7fc07fc0\n"
"v_mov_b32 v66, 0x7fc07fc0\n"
"v_mov_b32 v67, 0x7fc07fc0\n"
"v_mov_b32 v68, 0x7fc07fc0\n"
"v_mov_b32 v69, 0x7fc07fc0\n"
"v_mov_b32 v70, 0x7fc07fc0\n"
"v_mov_b32 v71, 0x7fc07fc0\n"
"s_nop 1\n"
"s_branch CH_LOOP%=\n"
"CH_FIN%=:\n"
"s_nop 7\n"
"s_waitcnt vmcnt(0) lgkmcnt(0)\n"
"CH_CODE_END:\n"
    : "+{v[0:7]}"(r0), "+{v[8:11]}"(r1a), "+{v[12:13]}"(r1b), "+{v14}"(r1c), "+{v[16:23]}"(r3), "+{v[24:31]}"(r2), "+{v[64:71]}"(r4)
    : "s"(srd_ws), "s"(srd_w), "s"(srd_v), "s"(srd_o), "s"(w), "s"(srd_m)
    :
"v32", "v33", "v34", "v35", "v36", "v37", "v38", "v39", "v40", "v41", "v42", "v43", "v44", "v45", "v46", "v47", "v48", "v49", "v50", "v51", "v52", "v53", "v54", "v55", "v56", "v57", "v58", "v59", "v60", "v61", "v62", "v63", "v72", "v73", "v74", "v75", "v76", "v77", "v78", "v79", "v80", "v81", "v82", "v83", "v84", "v85", "v86", "v87", "v88", "v89", "v90", "v91", "v92", "v93", "v94", "v95", "v96", "v97", "v98", "v99", "v100", "v101", "v102", "v103", "v104", "v105", "v106", "v107", "v108", "v109", "v110", "v111", "v112", "v113", "v114", "v115", "v116", "v117", "v118", "v119", "v120", "v121", "v122", "v123", "v124", "v125", "v126", "v127", "v128", "v129", "v130", "v131", "v132", "v133", "v134", "v135", "v136", "v137", "v138", "v139", "v140", "v141", "v142", "v143", "v144", "v145", "v146", "v147", "v148", "v149", "v150", "v151", "v152", "v153", "v154", "v155", "v156", "v157", "v158", "v159", "v160", "v161", "v162", "v163", "v164", "v165", "v166", "v167", "v168", "v169", "v170", "v171", "v172", "v173", "v174", "v175", "v176", "v177", "v178", "v179", "v180", "v181", "v182", "v183", "v184", "v185", "v186", "v187", "v188", "v189", "v190", "v191", "v192", "v193", "v194", "v195", "v196", "v197", "v198", "v199", "v200", "v201", "v202", "v203", "v204", "v205", "v206", "v207", "v208", "v209", "v210", "v211", "v212", "v213", "v214", "v215", "v216", "v217", "v218", "v219", "v220", "v221", "v222", "v223", "v224", "v225", "v226", "v227", "v228", "v229", "v230", "v231", "v232", "v233", "v234", "v235", "v236", "v237", "v238", "v239", "v240", "v241", "v242", "v243", "v244", "v245", "v246", "v247", "v248", "v249", "v250", "v251", "v252", "v253", "v254", "v255", "a0", "a1", "a2", "a3", "a4", "a5", "a6", "a7", "a8", "a9", "a10", "a11", "a12", "a13", "a14", "a15", "a16", "a17", "a18", "a19", "a20", "a21", "a22", "a23", "a24", "a25", "a26", "a27", "a28", "a29", "a30", "a31", "a32", "a33", "a34", "a35", "a36", "a37", "a38", "a39", "a40", "a41", "a42", "a43", "a44", "a45", "a46", "a47", "a48", "a49", "a50", "a51", "a52", "a53", "a54", "a55", "a56", "a57", "a58", "a59", "a60", "a61", "a62", "a63", "a64", "a65", "a66", "a67", "a68", "a69", "a70", "a71", "a72", "a73", "a74", "a75", "a76", "a77", "a78", "a79", "a80", "a81", "a82", "a83", "a84", "a85", "a86", "a87", "a88", "a89", "a90", "a91", "a92", "a93", "a94", "a95", "a96", "a97", "a98", "a99", "a100", "a101", "a102", "a103", "a104", "a105", "a106", "a107", "a108", "a109", "a110", "a111", "a112", "a113", "a114", "a115", "a116", "a117", "a118", "a119", "a120", "a121", "a122", "a123", "a124", "a125", "a126", "a127", "a128", "a129", "a130", "a131", "a132", "a133", "a134", "a135", "a136", "a137", "a138", "a139", "a140", "a141", "a142", "a143", "a144", "a145", "a146", "a147", "a148", "a149", "a150", "a151", "a152", "a153", "a154", "a155", "a156", "a157", "a158", "a159", "a160", "a161", "a162", "a163", "a164", "a165", "a166", "a167", "a168", "a169", "a170", "a171", "a172", "a173", "a174", "a175", "a176", "a177", "a178", "a179", "a180", "a181", "a182", "a183", "a184", "a185", "a186", "a187", "a188", "a189", "a190", "a191", "a192", "a193", "a194", "a195", "a196", "a197", "a198", "a199", "a200", "a201", "a202", "a203", "a204", "a205", "a206", "a207", "a208", "a209", "a210", "a211", "a212", "a213", "a214", "a215", "a216", "a217", "a218", "a219", "a220", "a221", "a222", "a223", "a224", "a225", "a226", "a227", "a228", "a229", "a230", "a231", "a232", "a233", "a234", "a235", "a236", "a237", "a238", "a239", "a240", "a241", "a242", "a243", "a244", "a245", "a246", "a247", "a248", "a249", "a250", "a251", "a252", "a253", "a254", "a255", "s36", "s37", "s38", "s39", "s40", "s41", "s42", "s43", "s44", "s45", "s46", "s47", "s48", "s49", "s50", "s51", "s52", "s53", "s54", "s55", "s56", "s57", "s58", "s59", "s60", "s61", "s62", "s63", "s64", "s65", "s66", "s67", "s68", "s69", "s70", "s71", "s72", "s73", "s74", "s75", "s76", "s77", "s78", "s79", "s80", "s81", "s82", "s83", "s84", "s85", "s86", "s87", "s88", "s89", "s90", "s91", "s92", "s93", "s94", "s95", "s96", "s97", "s98", "s99", "vcc", "scc", "m0", "memory"
  );
}

DEV unsigned rkey(unsigned bits, int row) {
  const unsigned u = bits & 0xffff0000u;
  return (((int)u < 0 ? ~u : u | 0x80000000u) & 0xffff0000u) | (unsigned)(2047 - row);
}
DEV float rval(unsigned k) { return __uint_as_float(((int)k < 0 ? k & 0x7fffffffu : ~k) & 0xffff0000u); }
DEV void top2(int w, rsrc_t rs, unsigned E, LAS char* lds, unsigned& budget, int nrows, unsigned& k0, unsigned& k1) {
  const int lane = lane_id(), tid = w * 64 + lane;
  const bool act = tid * 8 < nrows;
  v4u a[4];
  for (;;) {
    CFENCE();
    bool ok = true;
#pragma unroll
    for (int i = 0; i < 4; ++i) { a[i] = gld4(rs, C_R + tid * 64 + i * 16); ok = ok && a[i].y == E && a[i].w == E; }
    if (__all(ok || !act)) break;
    if (++budget > SPIN_CAP) break;
    __builtin_amdgcn_s_sleep(4);
  }
  CFENCE();
  k0 = 0; k1 = 0;
#pragma unroll
  for (int i = 0; i < 8; ++i) {
    const unsigned key = act ? rkey((i & 1) ? a[i >> 1].z : a[i >> 1].x, tid * 8 + i) : 0u;
    k1 = max(k1, min(k0, key)); k0 = max(k0, key);
  }
  const unsigned m0 = wmaxu(k0);
  const unsigned m1 = wmaxu(k0 == m0 ? k1 : k0);
  if (lane == 0) { ((LAS unsigned*)lds)[w * 2] = m0; ((LAS unsigned*)lds)[w * 2 + 1] = m1; }
  LBAR();
  k0 = 0; k1 = 0;
#pragma unroll
  for (int i = 0; i < 8; ++i) { const unsigned key = ((LAS unsigned*)lds)[i]; k1 = max(k1, min(k0, key)); k0 = max(k0, key); }
  LBAR();
}
DEV bool late_merge(rsrc_t rs, unsigned E, int s, unsigned& budget, unsigned& k0, unsigned& k1, v2u& a) {
  const int lane = lane_id(), rj = lane & 31;
  bool hi = (__ballot(a.y == E) >> 32) == 0xffffffffull;
  if (!hi) {
    for (;;) {
      CFENCE();
      a = __builtin_amdgcn_raw_buffer_load_b64(rs, C_RF + (s + (s == 62 ? lane >> 5 : 0) - SPEC_SEGS) * 4096 + rj * 128, 0, SC1);
      if ((unsigned)__ballot(a.y == E) == 0xffffffffu) break;
      if (++budget > SPIN_CAP) break;
      if (s < 61) __builtin_amdgcn_s_sleep(60); else __builtin_amdgcn_s_sleep(LM_SLEEP);
    }
    CFENCE();
  }
  const unsigned key = (lane >= 32) == hi ? rkey(a.x, s * 32 + rj) : 0u, m0 = wmaxu(key);
  if (s != 62) a.y = ~E;
  if (m0 < k1) return false;
  const unsigned m1 = wmaxu(key == m0 ? 0u : key);
  k1 = max(k1, min(k0, m0)); k0 = max(k0, m0); k1 = max(k1, m1);
  return true;
}
DEV void wait_spec(int w, rsrc_t rs, unsigned E, int lj, unsigned& budget) {
  if (w == 0) {
    for (;;) {
      CFENCE();
      if (gld1(rs, C_R + ((SPEC_SEGS - 1) * 32 + (lj & 31)) * 8 + 4) == E || ++budget > SPIN_CAP) break;
      __builtin_amdgcn_s_sleep(100);
    }
    __builtin_amdgcn_s_sleep(16);
  }
  LBAR();
}

template <int LPR> struct ModIn { v4u wm[2 * LPR]; u16 cv[2 * LPR]; };
template <int LPR> DEV void mod_load(ArgP Ap, int w, int col0, ModIn<LPR>& m) {
  constexpr int RP = 256 / LPR;
  const int tid = w * 64 + lane_id(), part = tid % LPR, rb = tid / LPR;
#pragma unroll
  for (int i = 0; i < 2 * LPR; ++i) {
    m.wm[i] = ild4(mkr(Ap->wmod, 512 * 6144), rb * 6144 + (col0 + part * 8) * 2, RP * i * 6144);
    m.cv[i] = __builtin_amdgcn_raw_buffer_load_b16(mkr(Ap->c, 1024), rb * 2, RP * i * 2, 0);
  }
}
template <int LPR> DEV void mod_pub(int w, rsrc_t rs, unsigned Ev, LAS char* lds, int col0, const ModIn<LPR>& m) {
  const int lane = lane_id(), tid = w * 64 + lane;
  float acc[8] = {0, 0, 0, 0, 0, 0, 0, 0};
#pragma unroll
  for (int i = 0; i < 2 * LPR; ++i) {
    const float cx = blo(m.cv[i]); const float s = rbf(cx * sigm(cx));
    float f[8]; unpack8(m.wm[i], f);
#pragma unroll
    for (int j = 0; j < 8; ++j) acc[j] += s * f[j];
  }
#pragma unroll
  for (int j = 0; j < 8; ++j) {
    float v = acc[j];
    if (LPR <= 1) v += dppf<0xB1>(v);
    if (LPR <= 2) v += dppf<0x4E>(v);
    v += dppf<0x124>(v); v += dppf<0x128>(v);
    acc[j] = xsum4(v);
  }
  if (lane < LPR) {
#pragma unroll
    for (int j = 0; j < 8; ++j) ((LAS float*)lds)[w * 64 + lane * 8 + j] = acc[j];
  }
  LBAR();
  if (tid < LPR * 4) {
    const LAS float* p = (LAS float*)lds + 2 * tid;
    gst2(rs, C_MOD + col0 * 4 + tid * 8, pk2(p[0] + p[64] + p[128] + p[192], p[1] + p[65] + p[129] + p[193]), Ev + 128u);
  }
  LBAR();
}
DEV void mod_get(rsrc_t rs, unsigned E, int which, unsigned& budget, float (&o)[8]) {
  const int lane = lane_id();
  v4u a, b;
  for (;;) {
    CFENCE();
    a = gld4(rs, C_MOD + which * 2048 + lane * 32); b = gld4(rs, C_MOD + which * 2048 + lane * 32 + 16);
    if (__all(a.y == E && a.w == E && b.y == E && b.w == E)) break;
    if (++budget > SPIN_CAP) break;
    __builtin_amdgcn_s_sleep(1);
  }
  unpack8((v4u){a.x, a.z, b.x, b.z}, o);
}
DEV void mod_a(int w, rsrc_t rs, unsigned E, LAS char* p, unsigned& budget, float (&sa)[8], float (&ca)[8]) {
  const int lane = lane_id(), tid = w * 64 + lane;
  v4u a;
  for (;;) {
    CFENCE();
    a = gld4(rs, C_MOD + tid * 16);
    if (__all(a.y == E && a.w == E)) break;
    if (++budget > SPIN_CAP) break;
    __builtin_amdgcn_s_sleep(MA_SLEEP);
  }
  *(LAS v2u*)(p + tid * 8) = (v2u){a.x, a.z};
  LBAR();
  unpack8(ldsr(p + lane * 16), sa); unpack8(ldsr(p + 1024 + lane * 16), ca);
#pragma unroll
  for (int i = 0; i < 8; ++i) ca[i] = rbf(1.f + ca[i]);
}
DEV v4u norm_row(v4u xr) {
  float v[8], o[8]; unpack8(xr, v); ln_row(v, o);
  return (v4u){pk2(o[0], o[1]), pk2(o[2], o[3]), pk2(o[4], o[5]), pk2(o[6], o[7])};
}
DEV v4u modulate(v4u nm, const float (&sa)[8], const float (&ca)[8]) {
  v4u o;
#pragma unroll
  for (int j = 0; j < 4; ++j) { const unsigned t = pk2(blo(nm[j]) * ca[2 * j], bhi(nm[j]) * ca[2 * j + 1]); o[j] = pk2(blo(t) + sa[2 * j], bhi(t) + sa[2 * j + 1]); }
  return o;
}

DEV unsigned role_L(ArgP Ap, int w, int lj, rsrc_t rs, unsigned Ev, LAS char* lds, unsigned& budget) {
  unsigned E;
  const int lane = lane_id(), tid = w * 64 + lane;
  const int n = lane & 15;
  {
    v4u xr[8];
#pragma unroll
    for (int i = 0; i < 8; ++i) xr[i] = ild4(mkr(Ap->x, 2048 * 1024), (lj * 32 + w * 8) * 1024 + lane * 16, i * 1024);
    if (lj >= 48 && w == 0) {
      gst4(rs, cmap((lj & 8 ? 54 : 49) * 8192 + (lj & 7) * 1024 + lane * 16, 0), (v4u){0, 0, 0, 0});
      gst4(rs, cmap((lj & 8 ? 54 : 49) * 8192 + (lj & 7) * 1024 + lane * 16, 8192), (v4u){0, 0, 0, 0});
    }
    E = epoch(Ev);
    if (lj >= 48) {
      if (w == 0) {
        DRAIN();
        unsigned tt[2] = {~0u, ~0u};
        for (int rep = 0; rep < VREPS; ++rep)
          for (int p = 0; p < 2; ++p) {
            const unsigned long long t0 = clk();
            v4u x = gld4(rs, cmap((lj & 8 ? 54 : 49) * 8192 + (lj & 7) * 1024 + rep * 128 + (lane & 7) * 16, p * 8192));
            asm volatile("s_waitcnt vmcnt(0)" : "+v"(x) :: "memory");
            tt[p] = min(tt[p], (unsigned)(clk() - t0));
          }
        if (lane == 0) gst1(rs, C_VOTE + (lj - 48) * 4, E + (tt[1] < tt[0] ? 2 : 1));
      }
      LBAR();
    }
#pragma unroll
    for (int i = 0; i < 8; ++i) { xr[i] = norm_row(xr[i]); asm volatile("" : "+v"(xr[i])); }
    float sa[8], ca[8];
    mod_a(w, rs, E, lds + 32768, budget, sa, ca);
#pragma unroll
    for (int i = 0; i < 8; ++i) {
      const int row = w * 8 + i;
      ldsw(lds + (((row >> 4) * 64 + lane) * 16 + ((row & 15) ^ n)) * 16, modulate(xr[i], sa, ca));
    }
    LBAR();
#pragma unroll
    for (int i = 0; i < 8; ++i) {
      const int q = i * 256 + tid;
      gst4(rs, WS_H1 + lj * 32768 + q * 16, ldsr(lds + ((q & ~15) + ((q & 15) ^ ((q >> 4) & 15))) * 16));
    }
    DRAIN();
    LBAR();
    if (tid == 0) gst1(rs, C_H1F + lj * 64, E + 1);
  }
  { ModIn<4> m; mod_load<4>(fresh(Ap), w, 1024 + lj * 32, m); mod_pub<4>(w, rs, E - 128u, lds, 1024 + lj * 32, m); }
  return E;
}

DEV void role_X(ArgP Ap, int w, int xj, rsrc_t rs, unsigned E, LAS char* lds, unsigned& budget) {
  const int lane = lane_id(), tid = w * 64 + lane;
  const int n = lane & 15, g = lane >> 4;
  unsigned k0, k1;
  int round = 0;
  v2u la = {0, ~E};
  rest_until(w, rs, C_KVF, E, 32, budget);
  if (xj < 22) {
    for (int r = 0; r < 65 - SPEC_SEGS; ++r) gst4(rs, C_Y + (r * 22 + xj) * 4096 + tid * 16, (v4u){~0u, ~0u, ~0u, ~0u});
    v4u fg[16], fu[16], fd[8][2];
    {
      const v4u sg = mksrd(Ap->wg, 512 * 2752), su = mksrd(Ap->wu, 512 * 2752);
      asm volatile("s_nop 4");
      const unsigned vo = (lane >> 3) * 2752 + xj * 128 + (lane & 7) * 16;
#pragma unroll
      for (int i = 0; i < 16; ++i) { dma16(sg, vo, (w * 16 + i) * (8 * 2752), (w * 16 + i) * 1024); dma16(su, vo, (w * 16 + i) * (8 * 2752), 65536 + (w * 16 + i) * 1024); }
#pragma unroll
      for (int t = 0; t < 2; ++t) {
        v4u a[8];
#pragma unroll
        for (int j = 0; j < 8; ++j) a[j] = ild4(mkr(Ap->wd, 1376 * 1024), (xj * 64 + 32 * t + 8 * g) * 1024 + 256 * w + 16 * n, j * 1024);
#pragma unroll
        for (int q = 0; q < 4; ++q)
#pragma unroll
          for (int m = 0; m < 4; ++m) { fd[2 * q][t][m] = lo2(a[2 * m][q], a[2 * m + 1][q]); fd[2 * q + 1][t][m] = hi2(a[2 * m][q], a[2 * m + 1][q]); }
      }
      DRAIN();
      LBAR();
      if (tid == 0) gst1(rs, C_YF + xj * 128, E + 1);
      LAS char* p = lds + (8 * g + (n >> 2)) * 128 + w * 32 + (n & 3) * 8;
#pragma unroll
      for (int t = 0; t < 16; ++t) {
        const v2u a = ldstr(p + t * 4096), b = ldstr(p + t * 4096 + 512), c = ldstr(p + 65536 + t * 4096), d = ldstr(p + 65536 + t * 4096 + 512);
        fg[t] = (v4u){a.x, a.y, b.x, b.y}; fu[t] = (v4u){c.x, c.y, d.x, d.y};
      }
    }
#pragma unroll
    for (int t = 0; t < 16; ++t) asm volatile("" : "+a"(fg[t]), "+a"(fu[t]), "+v"(fd[t >> 1][t & 1]));
    LAS char* const sc = lds + 131072;
    const bool live = xj * 64 + w * 16 < 1376;
    wait_spec(w, rs, E, xj, budget);
    top2(w, rs, E, sc, budget, SPEC_SEGS * 32, k0, k1);
    for (int s = SPEC_SEGS - 1; s < 64; ++s) {
      if (s >= SPEC_SEGS && !late_merge(rs, E, s, budget, k0, k1, la)) continue;
      const int row = 2047 - (int)(((n & 1) ? k1 : k0) & 2047u);
      v4u xb[16];
      for (;;) {
        CFENCE();
        unsigned mx = 0;
#pragma unroll
        for (int t = 0; t < 16; ++t) xb[t] = gld4(rs, WS_H2 + row * 1024 + t * 64 + g * 16);
        BATCH();
#pragma unroll
        for (int t = 0; t < 16; ++t) mx = max(mx, vmax4(xb[t]));
        if (__all(mx != ~0u) || ++budget > SPIN_CAP) break;
      }
      f32x4 dg = {0, 0, 0, 0}, du = {0, 0, 0, 0};
#pragma unroll
      for (int t = 0; t < 16; ++t) { dg = mf32(fg[t], xb[t], dg); du = mf32(fu[t], xb[t], du); }
      float act[4];
#pragma unroll
      for (int r = 0; r < 4; ++r) { const float gv = rbf(dg[r]); act[r] = live ? rbf(gv * sigm(gv)) * rbf(du[r]) : 0.f; }
      LAS char* ax = sc + 256 + (round & 1) * 256;
      if (n < 2) *(LAS v2u*)(ax + n * 128 + w * 32 + g * 8) = (v2u){pk2(act[0], act[1]), pk2(act[2], act[3])};
      LBAR();
      const v4u b0 = ldsr(ax + (n & 1) * 128 + g * 16), b1 = ldsr(ax + (n & 1) * 128 + 64 + g * 16);
      f32x4 dy[8];
#pragma unroll
      for (int q = 0; q < 8; ++q) dy[q] = mf32(fd[q][1], b1, mf32(fd[q][0], b0, (f32x4){0, 0, 0, 0}));
      if (n < 2) {
#pragma unroll
        for (int r = 0; r < 4; ++r) {
          const unsigned o = C_Y + (round * 22 + xj) * 4096 + n * 2048 + (128 * w + 32 * g + 8 * r) * 4;
          gst4(rs, o, clamp4((v4u){__float_as_uint(dy[0][r]), __float_as_uint(dy[1][r]), __float_as_uint(dy[2][r]), __float_as_uint(dy[3][r])}));
          gst4(rs, o + 16, clamp4((v4u){__float_as_uint(dy[4][r]), __float_as_uint(dy[5][r]), __float_as_uint(dy[6][r]), __float_as_uint(dy[7][r])}));
        }
      }
      ++round;
    }
  } else {
    const int fj = xj - 22, ct = w >> 1, d = fj * 64 + lane, hl = lane >> 5, d2 = fj * 64 + (lane & 31) * 2;
    const rsrc_t ro = mkr(Ap->out, 2048 * 1024);
    wait_spec(w, rs, E, xj, budget);
    wait_ge<2>(rs, C_YF + min(lane, 21) * 128, E, 1, budget);
    v2u gw;
    for (;;) {
      CFENCE();
      gw = __builtin_amdgcn_raw_buffer_load_b64(rs, C_MOD + (5 * 512 + d2) * 4, 0, SC1);
      if (__all(gw.y == E) || ++budget > SPIN_CAP) break;
    }
    top2(w, rs, E, lds, budget, SPEC_SEGS * 32, k0, k1);
    int prow = -1; unsigned plain = 0;
    for (int s = SPEC_SEGS - 1; s < 64; ++s) {
      if (s >= SPEC_SEGS && !late_merge(rs, E, s, budget, k0, k1, la)) continue;
      const unsigned kk = hl ? k1 : k0;
      const int row = 2047 - (int)(kk & 2047u);
      unsigned xw = 0, hw = 0;
      if (w == 0) {
        if (prow >= 0 && prow != 2047 - (int)(k0 & 2047u) && prow != 2047 - (int)(k1 & 2047u)) gst1(ro, prow * 1024 + d2 * 2, plain);
        for (;;) {
          CFENCE();
          if (__all(gld1(rs, C_R + row * 8 + 4) == E) || ++budget > SPIN_CAP) break;
        }
        CFENCE();
        xw = gld1(rs, WS_X1 + row * 1024 + d2 * 2); hw = gld1(rs, WS_H2 + row * 1024 + d2 * 2);
      }
      unsigned y[11];
      for (;;) {
        CFENCE();
        unsigned mx = 0;
#pragma unroll
        for (int i = 0; i < 11; ++i) y[i] = gld1(rs, C_Y + (round * 22 + (w & 1) * 11 + i) * 4096 + ct * 2048 + d * 4);
        BATCH();
#pragma unroll
        for (int i = 0; i < 11; ++i) mx = max(mx, y[i]);
        if (__all(mx != ~0u) || ++budget > SPIN_CAP) break;
        __builtin_amdgcn_s_sleep(2);
      }
      float sum = 0.f;
#pragma unroll
      for (int i = 0; i < 11; ++i) sum += __uint_as_float(y[i]);
      LAS float* fx = (LAS float*)(lds + 256 + (round & 1) * 1024);
      fx[tid] = sum;
      LBAR();
      if (w == 0) {
        const LAS float* p = fx + hl * 128 + (lane & 31) * 2;
        const float rs_ = rbf(sigm(rval(kk)));
        const float y0 = rbf(rbf(p[0] + p[64]) * rs_), y1 = rbf(rbf(p[1] + p[65]) * rs_);
        gst1(ro, row * 1024 + d2 * 2, pk2(blo(xw) + rbf(blo(gw.x) * rbf(blo(hw) + y0)), bhi(xw) + rbf(bhi(gw.x) * rbf(bhi(hw) + y1))));
        plain = pk2(blo(xw) + rbf(blo(gw.x) * blo(hw)), bhi(xw) + rbf(bhi(gw.x) * bhi(hw)));
        prow = row;
        DRAIN();
      }
      ++round;
    }
    if (fj == 0 && tid == 0) gst1(rs, C_EPOCH, E);
  }
}

DEV unsigned role_KV(ArgP Ap, int w, int wj, rsrc_t rs, LAS char* lds, unsigned& budget) {
  unsigned Ev;
  const int lane = lane_id(), tid = w * 64 + lane;
  const int n = lane & 15, g = lane >> 4;
  const int which = wj >> 6, h = (wj & 63) >> 1, hf = wj & 1;
  v4u b0[16], b1[16];
  v4u wf[4][16];
  {
    Ev = gld1(rs, C_EPOCH);
    if ((wj & 3) == 0) {
      const int cb = wj + w;
      v4u wm[8];
#pragma unroll
      for (int i = 0; i < 8; ++i) wm[i] = ild4(mkr(Ap->wmod, 512 * 6144), lane * (8 * 6144) + cb * 16, i * 6144);
      float cf[8], acc[8] = {0, 0, 0, 0, 0, 0, 0, 0};
      const v4u cw = ild4(mkr(Ap->c, 1024), lane * 16, 0);
      BATCH();
      unpack8(cw, cf);
#pragma unroll
      for (int i = 0; i < 8; ++i) {
        const float sv = rbf(cf[i] * sigm(cf[i]));
        float f[8]; unpack8(wm[i], f);
#pragma unroll
        for (int j = 0; j < 8; ++j) acc[j] += sv * f[j];
      }
#pragma unroll
      for (int j = 0; j < 8; ++j) acc[j] = wsum(acc[j]);
      const unsigned pv = lane & 2 ? (lane & 1 ? pk2(acc[6], acc[7]) : pk2(acc[4], acc[5])) : (lane & 1 ? pk2(acc[2], acc[3]) : pk2(acc[0], acc[1]));
      if (lane < 4) gst2(rs, C_MOD + cb * 32 + lane * 8, pv, Ev + 128u);
      LBAR();
    }
    const v4u sw = mksrd(which ? Ap->Wv : Ap->Wk, 4u << 20);
    const v4u sh = mksrd(which ? Ap->Wq : Ap->Wo, 4u << 20);
    asm volatile("s_nop 4");
    const int rr = lane >> 3;
    const unsigned vo = rr * 8192 + (h * 128 + hf * 64) * 2 + (((lane & 7) ^ ((rr >> 1) & 1)) * 16);
    auto bulk = [&](int ph, int i0, int i1) {
      if (ph == which) {
#pragma unroll
        for (int i = i0; i < i1; ++i) dma16(sw, vo, (w * 16 + i) * 65536, (w * 16 + i) * 1024);
      } else {
#pragma unroll
        for (int i = i0; i < i1; ++i) dma16(sh, lane * 16, (wj & 63) * 65536 + (w * 16 + i) * 1024, 65536 + w * 1024);
      }
    };
    if ((wj & 3) == 0) { bulk(which, 0, 16); bulk(which ^ 1, 0, 16); } else { bulk(0, 0, 16); bulk(1, 0, 16); }
    DRAIN();
    LBAR();
    const int q4 = n >> 2, p4 = n & 3;
#pragma unroll
    for (int q = 0; q < 4; ++q) {
      LAS char* p = lds + (8 * g + q4) * 128 + (((2 * p4 + (q >> 1)) ^ ((q4 >> 1) & 1)) * 16) + (q & 1) * 8;
#pragma unroll
      for (int t = 0; t < 16; ++t) { const v2u a = ldstr(p + t * 4096), b = ldstr(p + t * 4096 + 512); wf[q][t] = (v4u){a.x, a.y, b.x, b.y}; }
    }
  }
#pragma unroll
  for (int t = 0; t < 16; ++t) asm volatile("" : "+a"(wf[0][t]), "+a"(wf[1][t]), "+a"(wf[2][t]), "+v"(wf[3][t]));
  const unsigned E = epoch(Ev);
  const int tile = w & 1;
  const unsigned pos = which ? 16384 + (n >> 3) * 4096 + (4 * tile + (n & 3) + 8 * ((n >> 2) & 1)) * 16 : tile * 4096 + n * 16;
  const unsigned so = WS_KV + h * (2u << 20) + (near_class(rs, E, budget) ^ ((h & 1) ^ KVX) * 8192) + pos + (2 * hf + (g >> 1)) * 1024 + (g & 1) * 512;
  unsigned long long rdy = 0;
  auto fetch = [&](int i, v4u (&b)[16]) {
    const int seg = 2 * i + (w >> 1);
    while (!((rdy >> seg) & 1)) {
      CFENCE();
      rdy = __ballot(gld1(rs, C_H1F + lane * 64) == E + 1);
      if ((rdy >> seg) & 1) break;
      if (++budget > SPIN_CAP) { rdy = ~0ull; break; }
      __builtin_amdgcn_s_sleep(1);
    }
    CFENCE();
#pragma unroll
    for (int t = 0; t < 16; ++t) b[t] = gld4s(rs, lane * 16 + (t & 3) * 1024, WS_H1 + (4 * i + w) * 16384 + (t >> 2) * 4096);
    BATCH();
  };
  auto compute = [&](int i, const v4u (&b)[16]) {
    f32x4 d[4] = {{0, 0, 0, 0}, {0, 0, 0, 0}, {0, 0, 0, 0}, {0, 0, 0, 0}};
#pragma unroll
    for (int t = 0; t < 16; ++t)
#pragma unroll
      for (int q = 0; q < 4; ++q) d[q] = mf32(wf[q][t], b[t], d[q]);
    const unsigned o = so + (2 * i + (w >> 1)) * 32768;
    gst4(rs, o, (v4u){pk2(d[0][0], d[0][1]), pk2(d[0][2], d[0][3]), pk2(d[1][0], d[1][1]), pk2(d[1][2], d[1][3])});
    gst4(rs, o + 256, (v4u){pk2(d[2][0], d[2][1]), pk2(d[2][2], d[2][3]), pk2(d[3][0], d[3][1]), pk2(d[3][2], d[3][3])});
  };
  auto flag = [&](int v) { if (lane == 0) gst1(rs, C_KVF + wj * 64 + w * 4, E + v); };
  fetch(0, b0);
  compute(0, b0);
  DRAIN(); flag(1);
  fetch(1, b1);
  fetch(2, b0);
  for (int i = 1; i < 31; i += 2) {
    compute(i, b1);
    if (i + 2 < 32) fetch(i + 2, b1);
    asm volatile("s_waitcnt vmcnt(18)" ::: "memory");
    flag(i);
    compute(i + 1, b0);
    if (i + 3 < 32) { fetch(i + 3, b0); asm volatile("s_waitcnt vmcnt(18)" ::: "memory"); flag(i + 1); }
  }
  compute(31, b1);
  DRAIN(); flag(32);
  return E;
}

DEV unsigned pcls(unsigned nc, int s) { return nc ^ (((s & 1) ^ (s == 63) ^ PX) * 8192); }
constexpr int F_KV = 32768, F_QX = 98304, F_OX = 106496, F_VM = 114688;
constexpr int R_BUF = 131072, R_X = 137216, R_C = 139264;
DEV void role_F(ArgP Ap, int w, int fj, rsrc_t rs, unsigned E, LAS char* lds, unsigned& budget) {
  const int lane = lane_id(), tid = w * 64 + lane;
  const int n = lane & 15, g = lane >> 4;
  const int h = fj & 31, ln = fj >> 5, fl = ln < 3 ? ln : SEG_4 + 3;
  const unsigned nc = near_class(rs, E, budget);
  const unsigned ph = (h >> 3) * 16384 + (h & 7) * 1024;
  if (ln >= 2) {
#pragma unroll
    for (int i = 0; i < 8; ++i) gst4(rs, WS_PART + ((60 + ln) * 32 + 4 * i + w) * 65536 + pcls(nc, 60 + ln) + ph + lane * 16, (v4u){~0u, ~0u, ~0u, ~0u});
    if (ln == 3 && w == 0) gst4(rs, WS_H2 + (63 * 32 + h) * 1024 + lane * 16, (v4u){~0u, ~0u, ~0u, ~0u});
  }
  v4u wq[2][16], wo[8][4];
  {
    const unsigned qo = 8 * g * 8192 + (h * 128 + 32 * w + 2 * n) * 2;
#pragma unroll
    for (int hh = 0; hh < 2; ++hh) {
      unsigned a[8][8];
#pragma unroll
      for (int t = 0; t < 8; ++t)
#pragma unroll
        for (int j = 0; j < 8; ++j) a[t][j] = ild1(mkr(Ap->Wq, 512 * 8192), qo + (8 * hh + t) * 262144, j * 8192);
      BATCH();
#pragma unroll
      for (int t = 0; t < 8; ++t) {
#pragma unroll
        for (int m = 0; m < 4; ++m) { wq[0][8 * hh + t][m] = lo2(a[t][2 * m], a[t][2 * m + 1]); wq[1][8 * hh + t][m] = hi2(a[t][2 * m], a[t][2 * m + 1]); }
        asm volatile("" : "+a"(wq[0][8 * hh + t]), "+a"(wq[1][8 * hh + t]));
      }
    }
    const unsigned oo = (h * 128 + 8 * g) * 1024 + 256 * w + 16 * n;
    v4u a[4][8];
#pragma unroll
    for (int t = 0; t < 4; ++t)
#pragma unroll
      for (int j = 0; j < 8; ++j) a[t][j] = ild4(mkr(Ap->Wo, 4096 * 1024), oo + t * 32768 + (j & 3) * 1024, (j >> 2) * 4096);
    BATCH();
#pragma unroll
    for (int t = 0; t < 4; ++t)
#pragma unroll
      for (int q = 0; q < 4; ++q)
#pragma unroll
        for (int m = 0; m < 4; ++m) { wo[2 * q][t][m] = lo2(a[t][2 * m][q], a[t][2 * m + 1][q]); wo[2 * q + 1][t][m] = hi2(a[t][2 * m][q], a[t][2 * m + 1][q]); }
  }
  wait_ge<16>(rs, C_KVF + ((n >> 3) * 64 + 2 * h + ((n >> 2) & 1)) * 64 + (n & 3) * 4, E, 32, budget);
  wait_ge<4>(rs, C_H1F + lane * 64, E, 1, budget);
  DRAIN();
  LBAR();
  const int qt = w & 1, eh = w >> 1;
  const int q4 = n >> 2, p4 = n & 3;
  v4u km[4], vmr;
  unsigned kmseen = 0;
  const v4u sws = mksrd(Ap->ws, WS_BYTES);
  asm volatile("s_nop 4");
  auto fetch = [&](int s, unsigned dst) {
#pragma unroll
    for (int i = 0; i < 8; ++i) dma16s(sws, tid * 16, WS_H1 + s * 32768 + i * 4096, dst + i * 4096 + w * 1024);
#pragma unroll
    for (int i = 0; i < 4; ++i) dma16s(sws, tid * 16, WS_KV + h * (2u << 20) + (nc ^ ((h & 1) ^ KVX) * 8192) + s * 32768 + (i >> 1) * 16384 + (i & 1) * 4096, dst + F_KV + i * 4096 + w * 1024);
  };
  auto fetch_km = [&](int s) {
    const unsigned kb = WS_KM + (h * 64 + s) * 4096;
#pragma unroll
    for (int t = 0; t < 4; ++t) km[t] = gld4s(rs, (n & 7) * 256 + t * 64 + g * 16, kb);
    vmr = gld4s(rs, 2048 + (lane >> 3) * 256 + eh * 128 + (lane & 7) * 16, kb);
  };
  auto poll_km = [&]() {
    CFENCE();
    const unsigned v = gld1(rs, C_KMF + (2 * h + (lane & 1)) * 128);
    const unsigned a = __builtin_amdgcn_readlane(v, 0) - E, b = __builtin_amdgcn_readlane(v, 1) - E;
    kmseen = min(min(a > 64u ? 0u : a, b > 64u ? 0u : b), 63u);
    CFENCE();
  };
  unsigned cur = 0;
  fetch(fl, 0);
  DRAIN();
  poll_km();
  for (int s = fl, sn, done = 0; s < 64; done = s + 1, s = sn) {
    sn = s >= SEG_4 ? s + 4 : s + 3 < SEG_4 ? s + 3 : SEG_4 + ln;
#pragma unroll
    for (int t = 0; t < 16; ++t) asm volatile("" : "+a"(wq[0][t]), "+a"(wq[1][t]));
#pragma unroll
    for (int t = 0; t < 4; ++t) asm volatile("" : "+a"(wo[0][t]), "+a"(wo[1][t]), "+a"(wo[2][t]), "+a"(wo[3][t]), "+v"(wo[4][t]), "+v"(wo[5][t]), "+v"(wo[6][t]), "+v"(wo[7][t]));
    asm volatile("s_waitcnt vmcnt(8)" ::: "memory");
    LBAR();
    LAS char* const in = lds + cur;
    cur = 49152 - cur;
    const bool early = kmseen > (unsigned)s;
    if (early) fetch_km(s);
    {
      f32x4 d[2][2] = {{{0, 0, 0, 0}, {0, 0, 0, 0}}, {{0, 0, 0, 0}, {0, 0, 0, 0}}};
      v4u hb[2][4];
#pragma unroll
      for (int i = 0; i < 4; ++i) hb[0][i] = ldsr(in + (i & 1) * 16384 + (i >> 1) * 1024 + lane * 16);
#pragma unroll
      for (int gq = 0; gq < 8; ++gq) {
        if (gq < 7) {
#pragma unroll
          for (int i = 0; i < 4; ++i) hb[(gq + 1) & 1][i] = ldsr(in + (i & 1) * 16384 + (2 * gq + 2 + (i >> 1)) * 1024 + lane * 16);
        }
        __builtin_amdgcn_sched_barrier(0);
#pragma unroll
        for (int i = 0; i < 4; ++i) {
          d[i & 1][0] = mf32(wq[0][2 * gq + (i >> 1)], hb[gq & 1][i], d[i & 1][0]); d[i & 1][1] = mf32(wq[1][2 * gq + (i >> 1)], hb[gq & 1][i], d[i & 1][1]);
        }
        asm volatile("" : "+a"(d[0][0]), "+a"(d[0][1]), "+a"(d[1][0]), "+a"(d[1][1]));
        __builtin_amdgcn_sched_barrier(0);
      }
#pragma unroll
      for (int T = 0; T < 2; ++T)
        ldsw(lds + F_QX + (T * 4 + w) * 1024 + lane * 16, (v4u){pk2(d[T][0][0], d[T][1][0]), pk2(d[T][0][1], d[T][1][1]), pk2(d[T][0][2], d[T][1][2]), pk2(d[T][0][3], d[T][1][3])});
    }
    asm volatile("s_waitcnt vmcnt(0)" : "+v"(km[0]), "+v"(km[1]), "+v"(km[2]), "+v"(km[3]), "+v"(vmr) :: "memory");
    LBAR();
    if (tid == 0) gst1(rs, C_PF + fj * 128, E + done);
    if (sn < 64) fetch(sn, cur);
    v4u qf[4];
    f32x4 s0 = {0, 0, 0, 0}, s1 = {0, 0, 0, 0}, sm = {0, 0, 0, 0};
#pragma unroll
    for (int t = 0; t < 4; ++t) {
      qf[t] = ldsr(lds + F_QX + (qt * 4 + t) * 1024 + lane * 16);
      s0 = mf32(ldsr(in + F_KV + t * 1024 + lane * 16), qf[t], s0);
      s1 = mf32(ldsr(in + F_KV + 4096 + t * 1024 + lane * 16), qf[t], s1);
    }
    v2u vt[4][2];
#pragma unroll
    for (int e = 0; e < 4; ++e)
#pragma unroll
      for (int T = 0; T < 2; ++T)
        vt[e][T] = ldstr(in + F_KV + 8192 + (g >> 1) * 4096 + (2 * eh + (e >> 1)) * 1024 + p4 * 256 + (4 * T + q4 + 8 * (g & 1)) * 16 + (e & 1) * 8);
    const float scale = 0.08838834764831845f;
    float sv[12];
#pragma unroll
    for (int r = 0; r < 4; ++r) {
      const int key = 4 * g + r, qi = 16 * qt + n;
      const unsigned u = pk2(s0[r], s1[r]);
      unsigned v = pk2(blo(u) * scale, bhi(u) * scale);
      asm volatile("" : "+v"(v));
      sv[4 + r] = key <= qi ? blo(v) : -INFINITY;
      sv[8 + r] = key + 16 <= qi ? bhi(v) : -INFINITY;
    }
    asm volatile("" :: "v"(sv[4]), "v"(sv[8]), "v"(vt[0][0]), "v"(vt[3][1]));
    if (s == 63) wait_ge<8>(rs, C_PF + (96 + (lane & 31)) * 128, E, 0, budget);
    if (!early) {
      for (;;) {
        poll_km();
        if (kmseen + (s >= 62 ? 1u : 0u) > (unsigned)s || ++budget > SPIN_CAP) break;
        if (kmseen + 2 < (unsigned)s) __builtin_amdgcn_s_sleep(100); else if (s < 61) __builtin_amdgcn_s_sleep(KM_SLEEP); else __builtin_amdgcn_s_sleep(4);
      }
      if (s == 63) __builtin_amdgcn_s_sleep(KM63_DELAY);
      if (s == 62) __builtin_amdgcn_s_sleep(KM62_DELAY);
      for (;;) {
        CFENCE();
        fetch_km(s);
        if (s < 62 || __all(max(max(vmax4(km[0]), vmax4(km[1])), max(max(vmax4(km[2]), vmax4(km[3])), vmax4(vmr))) != ~0u) || ++budget > SPIN_CAP) break;
        __builtin_amdgcn_s_sleep(1);
      }
    }
    ldsw(lds + F_VM + w * 1152 + (lane >> 3) * 144 + (lane & 7) * 16, vmr);
    CFENCE();
#pragma unroll
    for (int t = 0; t < 4; ++t) sm = mf32(km[t], qf[t], sm);
    v2u vm[4];
#pragma unroll
    for (int e = 0; e < 4; ++e) vm[e] = ldstr(lds + F_VM + w * 1152 + ((4 * g + q4) & 7) * 144 + (e >> 1) * 64 + (e & 1) * 32 + p4 * 8);
#pragma unroll
    for (int r = 0; r < 4; r += 2) {
      const unsigned u = pk2(sm[r], sm[r + 1]);
      unsigned v = pk2(blo(u) * scale, bhi(u) * scale);
      v = g < 2 ? v : 0xff80ff80u;
      sv[r] = blo(v); sv[r + 1] = bhi(v);
    }
    float m = sv[0];
#pragma unroll
    for (int i = 1; i < 12; ++i) m = fmaxf(m, sv[i]);
    m = xmax4(m);
    float l = 0.f;
#pragma unroll
    for (int i = 0; i < 12; ++i) { sv[i] = __builtin_amdgcn_exp2f((sv[i] - m) * 1.4426950408889634f); l += sv[i]; }
    const float il = __builtin_amdgcn_rcpf(xsum4(l));
    const v4u pa = {pk2(sv[0] * il, sv[1] * il), pk2(sv[2] * il, sv[3] * il), pk2(sv[4] * il, sv[5] * il), pk2(sv[6] * il, sv[7] * il)};
    const v2u pb = {pk2(sv[8] * il, sv[9] * il), pk2(sv[10] * il, sv[11] * il)};
    f32x4 o[4];
#pragma unroll
    for (int e = 0; e < 4; ++e) {
      o[e] = mf32((v4u){vm[e].x, vm[e].y, vt[e][0].x, vt[e][0].y}, pa, (f32x4){0, 0, 0, 0});
      o[e] = mf16(vt[e][1], pb, o[e]);
    }
#pragma unroll
    for (int t = 0; t < 2; ++t)
      ldsw(lds + F_OX + (qt * 4 + 2 * eh + t) * 1024 + lane * 16, (v4u){pk2(o[2 * t][0], o[2 * t][1]), pk2(o[2 * t][2], o[2 * t][3]), pk2(o[2 * t + 1][0], o[2 * t + 1][1]), pk2(o[2 * t + 1][2], o[2 * t + 1][3])});
    LBAR();
    if (s == 62 && tid == 0) gst1(rs, C_PF + fj * 128, E + s + 1);
    v4u ob[8];
#pragma unroll
    for (int i = 0; i < 8; ++i) ob[i] = ldsr(lds + F_OX + i * 1024 + lane * 16);
    __builtin_amdgcn_sched_barrier(0);
#pragma unroll
    for (int T = 0; T < 2; ++T) {
      f32x4 d[8];
#pragma unroll
      for (int q = 0; q < 8; ++q) d[q] = (f32x4){0, 0, 0, 0};
#pragma unroll
      for (int t = 0; t < 4; ++t) {
#pragma unroll
        for (int q = 0; q < 8; ++q) d[q] = mf32(wo[q][t], ob[T * 4 + t], d[q]);
      }
      const unsigned o2 = WS_PART + (s * 32 + 16 * T + n) * 65536 + pcls(nc, s) + ph + w * 256 + g * 16;
#pragma unroll
      for (int r = 0; r < 4; ++r) gst4(rs, o2 + r * 64, clamp4((v4u){pk2(d[0][r], d[1][r]), pk2(d[2][r], d[3][r]), pk2(d[4][r], d[5][r]), pk2(d[6][r], d[7][r])}));
    }
    if (s >= 52 && s < 62) {
      DRAIN();
      LBAR();
      if (tid == 0) gst1(rs, C_PF + fj * 128, E + s + 1);
    }
  }
}

DEV void role_R(ArgP Ap, int w, int rj, bool tail, int st, unsigned ncls, rsrc_t rs, unsigned E, LAS char* lds, unsigned& budget, const float (&ga)[8], const float (&sb)[8], const float (&cb)[8], const float (&gb)[8], const float (&wr)[8]) {
  const int lane = lane_id();
  const int n = lane & 15, g = lane >> 4;
  const rsrc_t ro = mkr(Ap->out, 2048 * 1024);
  v4u sel;
  { const unsigned one = (n & 3) < 2 ? (0x3f80u << (16 * (n & 1))) : 0u;
#pragma unroll
    for (int k = 0; k < 4; ++k) sel[k] = (n >> 2) == k ? one : 0u; }
  LAS char* buf = lds + R_BUF + (tail ? 0 : w * 1024);
  const unsigned nc = ncls;
  if (!tail) {
    for (int s = SPEC_SEGS + ((w - SPEC_SEGS) & 3); s < 63; s += 4) gst4(rs, WS_H2 + (s * 32 + rj) * 1024 + lane * 16, (v4u){~0u, ~0u, ~0u, ~0u});
  }
  for (int s = tail ? st : w; s < (tail ? st + 1 : 63); s += 4) {
    if (!tail && s == SEG_W) continue;
    const int row = s * 32 + rj;
    const v4u xr = tail ? ldsr(lds + R_X + (s & 1) * 1024 + lane * 16) : ild4(mkr(Ap->x, 2048 * 1024), lane * 16, row * 1024);
    if (tail) {
      if (s < 63) rest_until(w, rs, C_PF + (((s + 1) % 3) * 32 + rj) * 128, E, s + 5, budget);
      if (s < 63) wait_ge<16>(rs, C_PF + ((s % 3) * 32 + (lane & 31)) * 128, E, s + 1, budget);
      v4u p[8];
      for (;;) {
        CFENCE();
        unsigned mx = 0;
#pragma unroll
        for (int m = 0; m < 8; ++m) p[m] = gld4s(rs, g * 1024 + w * 256 + n * 16, WS_PART + row * 65536 + pcls(nc, s) + (m >> 1) * 16384 + (m & 1) * 4096);
        BATCH();
#pragma unroll
        for (int m = 0; m < 8; ++m) mx = max(mx, vmax4(p[m]));
        if (__all(mx != ~0u) || ++budget > SPIN_CAP) break;
      }
      f32x4 d = {0, 0, 0, 0};
#pragma unroll
      for (int m = 0; m < 8; ++m) d = mf32(sel, p[m], d);
      *(LAS unsigned*)(buf + (128 * w + 32 * (n & 3) + 8 * (n >> 2) + 2 * g) * 2) = pk2(d[0], d[1]);
      LBAR();
      if (w) break;
    } else {
      wait_ge<2>(rs, C_PF + ((s < SEG_4 ? s % 3 : s & 3) * 32 + (lane & 31)) * 128, E, s + 1, budget);
      if (s == 62) __builtin_amdgcn_s_sleep(P63_DELAY);
      v4u p[4][8];
      for (;;) {
        CFENCE();
        unsigned mx = 0;
#pragma unroll
        for (int c = 0; c < 4; ++c)
#pragma unroll
          for (int m = 0; m < 8; ++m) p[c][m] = gld4s(rs, g * 1024 + c * 256 + n * 16, WS_PART + row * 65536 + pcls(nc, s) + (m >> 1) * 16384 + (m & 1) * 4096);
        BATCH();
        if (s < 62) break;
#pragma unroll
        for (int c = 0; c < 4; ++c)
#pragma unroll
          for (int m = 0; m < 8; ++m) mx = max(mx, vmax4(p[c][m]));
        if (__all(mx != ~0u) || ++budget > SPIN_CAP) break;
      }
#pragma unroll
      for (int c = 0; c < 4; ++c) {
        f32x4 d = {0, 0, 0, 0};
#pragma unroll
        for (int m = 0; m < 8; ++m) d = mf32(sel, p[c][m], d);
        *(LAS unsigned*)(buf + (128 * c + 32 * (n & 3) + 8 * (n >> 2) + 2 * g) * 2) = pk2(d[0], d[1]);
      }
    }
    CFENCE();
    float a[8], x1[8], nm[8];
    unpack8(ldsr(buf + lane * 16), a); unpack8(xr, x1);
#pragma unroll
    for (int i = 0; i < 8; ++i) x1[i] = rbf(x1[i] + rbf(ga[i] * a[i]));
    ln_row(x1, nm);
    float r = 0.f, h2[8];
#pragma unroll
    for (int i = 0; i < 8; ++i) { h2[i] = rbf(rbf(nm[i] * cb[i]) + sb[i]); r += h2[i] * wr[i]; }
    gst4(rs, WS_H2 + row * 1024 + lane * 16, clamp4((v4u){pk2(h2[0], h2[1]), pk2(h2[2], h2[3]), pk2(h2[4], h2[5]), pk2(h2[6], h2[7])}));
    r = wsum(r);
    const unsigned rb = pk2(0.f, r);
    if (s >= SPEC_SEGS && lane == 0) gst2(rs, C_RF + (s - SPEC_SEGS) * 4096 + rj * 128, rb, E);
    gst4(rs, WS_X1 + row * 1024 + lane * 16, (v4u){pk2(x1[0], x1[1]), pk2(x1[2], x1[3]), pk2(x1[4], x1[5]), pk2(x1[6], x1[7])});
    v4u o;
#pragma unroll
    for (int i = 0; i < 4; ++i) o[i] = pk2(x1[2 * i] + rbf(gb[2 * i] * h2[2 * i]), x1[2 * i + 1] + rbf(gb[2 * i + 1] * h2[2 * i + 1]));
    gst4(ro, row * 1024 + lane * 16, o);
    DRAIN();
    if (lane == 0) gst2(rs, C_R + row * 8, rb, E);
  }
}

__global__ void __launch_bounds__(256, 1) fused_kernel(Args A) {
  extern __shared__ __attribute__((aligned(16))) char smem[];
  LAS char* lds = (LAS char*)smem;
  const ArgP Ap = (ArgP)__builtin_amdgcn_kernarg_segment_ptr();
  const int b = blockIdx.x, xg = b & 7, k = b >> 3, w = __builtin_amdgcn_readfirstlane(threadIdx.x >> 6);
  const rsrc_t rs = __builtin_amdgcn_make_buffer_rsrc(Ap->ws, 0, WS_BYTES, 0x00020000);
  if ((k & 3) == 3) {
    asm volatile("s_getpc_b64 s[40:41]\n.Lcpf:\n\ts_mov_b32 s42, CH_CODE_END-.Lcpf\n\ts_mov_b32 s43, 0x00020000\n\ts_and_b32 s41, s41, 0xffff\n\t"
                 "s_mov_b32 m0, %1\n\ts_mov_b32 s44, 0x8000\n\ts_mov_b32 s45, 0x10000\n\ts_mov_b32 s46, 0x18000\n\t"
                 "buffer_load_dword %0, s[40:43], 0 offen lds\n\tbuffer_load_dword %0, s[40:43], s44 offen lds\n\t"
                 "buffer_load_dword %0, s[40:43], s45 offen lds\n\tbuffer_load_dword %0, s[40:43], s46 offen lds"
                 :: "v"(threadIdx.x * 128), "i"(L_END - 256) : "s40", "s41", "s42", "s43", "s44", "s45", "s46", "scc", "m0", "memory");
  }
  if (xg < 4 && k < 16) { chain_role(fresh(Ap), w, xg * 16 + k, rs
); __builtin_amdgcn_endpgm(); }
  unsigned budget = 0, E;
  int fj;
  if (xg < 4) {
    const int lj = (k - 16) * 4 + xg;
    E = role_L(fresh(Ap), w, lj, rs, gld1(rs, C_EPOCH), lds, budget);
    if (lj >= 32) { if (lj < 62) role_X(fresh(Ap), w, lj - 32, rs, E, lds, budget); __builtin_amdgcn_endpgm(); }
    fj = 96 + lj;
  } else {
    E = role_KV(fresh(Ap), w, (xg - 4) * 32 + k, rs, lds, budget);
    fj = k * 4 + (xg - 4);
    if (fj >= 96) fj += 32;
  }
  if (fj >= 96) {
    if (fj < 128 && w == 0) {
      ldsw(lds + R_X + lane_id() * 16, ild4(mkr(fresh(Ap)->x, 2048 * 1024), lane_id() * 16, (SEG_W * 32 + (fj & 31)) * 1024));
      ldsw(lds + R_X + 1024 + lane_id() * 16, ild4(mkr(fresh(Ap)->x, 2048 * 1024), lane_id() * 16, (63 * 32 + (fj & 31)) * 1024));
    }
    float v[8];
    LAS char* p = lds + R_C + lane_id() * 32;
#pragma unroll
    for (int i = 0; i < 5; ++i) {
      if (i < 4) mod_get(rs, E, 2 + i, budget, v); else unpack8(ild4(mkr(fresh(Ap)->wr, 1024), lane_id() * 16, 0), v);
#pragma unroll
      for (int j = 0; j < 8; ++j) ((LAS float*)(p + i * 2048))[j] = i == 2 ? rbf(1.f + v[j]) : v[j];
    }
  }
  const unsigned ncls = near_class(rs, E, budget);
  for (int st = SEG_W;; st = 63) {
    if (fj < 96 || st == 63) role_F(fresh(Ap), opq(w), opq(fj), rs, E, lds, budget);
    if (fj >= 96) {
      float c[5][8];
      CFENCE();
      const LAS char* p = lds + R_C + lane_id() * 32;
#pragma unroll
      for (int i = 0; i < 5; ++i)
#pragma unroll
        for (int j = 0; j < 8; ++j) c[i][j] = ((const LAS float*)(p + i * 2048))[j];
      role_R(fresh(Ap), opq(w), opq(fj) & 31, fj < 128, st, ncls, rs, E, lds, budget, c[0], c[1], c[2], c[3], c[4]);
    }
    if (fj < 96 || fj >= 128 || st == 63) break;
  }
}

extern "C" void kernel_launch(void* const* d_in, const int* in_sizes, int n_in, void* d_out, int out_size, void* d_ws, size_t ws_size, hipStream_t stream) {
  Args A;
  A.x = (const u16*)d_in[0]; A.mem0 = (const u16*)d_in[1]; A.Wq = (const u16*)d_in[2]; A.Wk = (const u16*)d_in[3]; A.Wv = (const u16*)d_in[4]; A.Wo = (const u16*)d_in[5];
  A.wr = (const u16*)d_in[8]; A.wg = (const u16*)d_in[9]; A.wu = (const u16*)d_in[10]; A.wd = (const u16*)d_in[11]; A.c = (const u16*)d_in[12]; A.wmod = (const u16*)d_in[13];
  A.out = (u16*)d_out; A.ws = (char*)d_ws;
  (void)hipFuncSetAttribute((const void*)fused_kernel, hipFuncAttributeMaxDynamicSharedMemorySize, L_END);
  fused_kernel<<<256, 256, L_END, stream>>>(A);
}
```
